```python
import torch

def _softmax(x, axis=-1):
    return torch.softmax(x, dim=axis)
def _gelu(x):
    return torch.nn.functional.gelu(x, approximate="tanh")
def _silu(x):
    return torch.nn.functional.silu(x)
def _sigmoid(x):
    return torch.sigmoid(x)
def _layernorm(x, axis=-1, eps=1e-5):
    m = x.mean(dim=axis, keepdim=True)
    v = x.var(dim=axis, keepdim=True, unbiased=False)
    return (x - m) / torch.sqrt(v + eps)
def _scatter_add(tgt, idx, src, axis=0):
    return tgt.clone().index_add_(axis, idx, src)
def _segment_sum(src, seg, n):
    out = torch.zeros((n,) + src.shape[1:], dtype=src.dtype)
    return out.index_add_(0, seg, src)
def _pad(x, pw):
    flat = tuple(v for pair in reversed(pw) for v in pair)
    return torch.nn.functional.pad(x, flat)

kernel_name = 'gated_mix__linear_attn_block__linear_attn_block__prompt'

def setup_inputs():

    _w = (lambda a: (lambda t: t.to(torch.bfloat16) if t.is_floating_point() else t)(torch.as_tensor(a)))
    def _A_setup():
        B, S, D = 1, 2048, 512
        return {
            "x": _w((lambda *s: torch.randn(*s, dtype=torch.bfloat16) * 0.05)(B, S, D)),
            "wqkv": _w((lambda *s: torch.randn(*s, dtype=torch.bfloat16) * 0.8)(D, 3 * D)),
            "wo": _w((lambda *s: torch.randn(*s, dtype=torch.bfloat16) * 0.05)(D, D)),
        }
    _A_in = _A_setup()

    def _B_setup():
        B, S, D = 1, 2048, 512
        return {
            "x": _w((lambda *s: torch.randn(*s, dtype=torch.bfloat16) * 0.05)(B, S, D)),
            "wqkv": _w((lambda *s: torch.randn(*s, dtype=torch.bfloat16) * 0.8)(D, 3 * D)),
            "wo": _w((lambda *s: torch.randn(*s, dtype=torch.bfloat16) * 0.05)(D, D)),
        }
    _B_in = _B_setup()

    return {
        "A_x": _A_in["x"],
        "A_wqkv": _A_in["wqkv"],
        "A_wo": _A_in["wo"],
        "B_x": _B_in["x"],
        "B_wqkv": _B_in["wqkv"],
        "B_wo": _B_in["wo"],
        "gate_w": _w((lambda *s: torch.randn(*s, dtype=torch.bfloat16) * 0.05)(512) * 16.0),
    }

def reference(A_x, A_wqkv, A_wo, B_x, B_wqkv, B_wo, gate_w):
    def _A_ref(x, wqkv, wo):
        B, S, D = x.shape
        H = 32; Dh = D // H
        qkv = (lambda x, *d: x.permute(*d))((x @ wqkv).reshape(B, S, 3, H, Dh), 2, 0, 3, 1, 4)
        q, k, v = qkv[0], qkv[1], qkv[2]
        phi = lambda t: torch.where(t > 0, t + 1, torch.exp(t)) + 1e-6
        pq, pk = phi(q), phi(k)
        kv = torch.einsum("bhsd,bhse->bhde", pk, v)
        z = torch.einsum("bhsd->bhd", pk)
        num = torch.einsum("bhqd,bhde->bhqe", pq, kv)
        den = torch.einsum("bhqd,bhd->bhq", pq, z)[..., None] + 1e-6
        o = num / den
        return (lambda x, *d: x.permute(*d))(o, 0, 2, 1, 3).reshape(B, S, D) @ wo

    def _B_ref(x, wqkv, wo):
        B, S, D = x.shape
        H = 32; Dh = D // H
        qkv = (lambda x, *d: x.permute(*d))((x @ wqkv).reshape(B, S, 3, H, Dh), 2, 0, 3, 1, 4)
        q, k, v = qkv[0], qkv[1], qkv[2]
        phi = lambda t: torch.where(t > 0, t + 1, torch.exp(t)) + 1e-6
        pq, pk = phi(q), phi(k)
        kv = torch.einsum("bhsd,bhse->bhde", pk, v)
        z = torch.einsum("bhsd->bhd", pk)
        num = torch.einsum("bhqd,bhde->bhqe", pq, kv)
        den = torch.einsum("bhqd,bhd->bhq", pq, z)[..., None] + 1e-6
        o = num / den
        return (lambda x, *d: x.permute(*d))(o, 0, 2, 1, 3).reshape(B, S, D) @ wo

    x = A_x
    g = _sigmoid((x * gate_w).sum(-1, keepdims=True))
    a = _A_ref(x, A_wqkv, A_wo)
    b = _B_ref(x, B_wqkv, B_wo)
    return g * a + (1 - g) * b
```

```cpp
#include <hip/hip_runtime.h>
#include <hip/hip_bf16.h>
typedef unsigned short u16;
__device__ inline float b2f(u16 x){ return __uint_as_float(((unsigned)x)<<16); }
__device__ inline u16 f2b(float f){ unsigned u=__float_as_uint(f); u += 0x7fffu + ((u>>16)&1u); return (u16)(u>>16);}
__device__ inline float rb(float f){ return b2f(f2b(f)); }
constexpr int S=2048, D=512, H=32, DH=16;
__global__ void k_qkv(const u16* x, const u16* wA, const u16* wB, float* qkv){
  int br = blockIdx.z; const u16* w = br? wB: wA;
  int n = blockIdx.x*256+threadIdx.x; int s = blockIdx.y;
  float acc=0; for(int k=0;k<D;k++) acc += b2f(x[s*D+k])*b2f(w[k*3*D+n]);
  float t = rb(acc);
  if(n<2*D){ float p = t>0? rb(t+1.f): rb(__expf(t)); t = rb(p+1e-6f);}
  qkv[((size_t)br*S+s)*3*D+n]=t;
}
__global__ void k_kv(const float* qkv, float* kv, float* z){
  int br=blockIdx.y, h=blockIdx.x; int d=threadIdx.x>>4, e=threadIdx.x&15;
  float acc=0, zz=0;
  for(int s=0;s<S;s++){ const float* r=qkv+((size_t)br*S+s)*3*D; float pk=r[D+h*16+d]; acc+=pk*r[2*D+h*16+e]; zz+=pk;}
  kv[((br*H+h)*16+d)*16+e]=rb(acc); if(e==0) z[(br*H+h)*16+d]=rb(zz);
}
__global__ void k_o(const float* qkv, const float* kv, const float* z, float* o){
  int br=blockIdx.y, s=blockIdx.x; int f=threadIdx.x;
  int h=f>>4,e=f&15; const float* r=qkv+((size_t)br*S+s)*3*D+h*16;
  float num=0,den=0; for(int d=0;d<16;d++){ num+=r[d]*kv[((br*H+h)*16+d)*16+e]; den+=r[d]*z[(br*H+h)*16+d]; }
  num=rb(num); den=rb(rb(den)+1e-6f); o[((size_t)br*S+s)*D+f]=rb(num/den);
}
__global__ void k_out(const float* o, const u16* x, const u16* gw, const u16* woA, const u16* woB, u16* out){
  int s=blockIdx.x; int n=threadIdx.x;
  float g=0; for(int k=0;k<D;k++) g+=rb(b2f(x[s*D+k])*b2f(gw[k])); g=rb(g); g=rb(1.f/(1.f+__expf(-g)));
  float a=0,b=0; for(int k=0;k<D;k++){ a+=o[(size_t)s*D+k]*b2f(woA[k*D+n]); b+=o[((size_t)S+s)*D+k]*b2f(woB[k*D+n]); }
  a=rb(a); b=rb(b);
  out[s*D+n]=f2b(rb(g*a)+rb(rb(1.f-g)*b));
}
extern "C" void kernel_launch(void* const* d_in, const int* in_sizes, int n_in, void* d_out, int out_size, void* d_ws, size_t ws_size, hipStream_t stream) {
  const u16* x=(const u16*)d_in[0]; const u16* wA=(const u16*)d_in[1]; const u16* woA=(const u16*)d_in[2];
  const u16* wB=(const u16*)d_in[4]; const u16* woB=(const u16*)d_in[5]; const u16* gw=(const u16*)d_in[6];
  float* qkv=(float*)d_ws; float* kv=qkv+(size_t)2*S*3*D; float* z=kv+2*H*256; float* o=z+2*H*16;
  k_qkv<<<dim3(6,S,2),256,0,stream>>>(x,wA,wB,qkv);
  k_kv<<<dim3(H,2),256,0,stream>>>(qkv,kv,z);
  k_o<<<dim3(S,2),512,0,stream>>>(qkv,kv,z,o);
  k_out<<<S,512,0,stream>>>(o,x,gw,woA,woB,(u16*)d_out);
}
```

```cpp
#include <hip/hip_runtime.h>
#include <hip/hip_bf16.h>
#include <stdint.h>

typedef unsigned short u16;
typedef unsigned long long u64;
typedef short bf16x8 __attribute__((ext_vector_type(8)));
typedef short s16x4 __attribute__((ext_vector_type(4)));
typedef float f32x16 __attribute__((ext_vector_type(16)));
typedef float f32x4 __attribute__((ext_vector_type(4)));
typedef float f32x2 __attribute__((ext_vector_type(2)));
typedef unsigned u32x4 __attribute__((ext_vector_type(4)));
typedef unsigned u32x2 __attribute__((ext_vector_type(2)));
typedef __bf16 bf16x2_t __attribute__((ext_vector_type(2)));
#define GAS __attribute__((address_space(1)))
#define LAS __attribute__((address_space(3)))
typedef GAS u64 gu64;
typedef GAS unsigned gu32;
typedef LAS char* ldsp;
#define DI __device__ __forceinline__
#define RLX_AGENT __ATOMIC_RELAXED, __HIP_MEMORY_SCOPE_AGENT
#define MFMA(a, b, c) __builtin_amdgcn_mfma_f32_32x32x16_bf16((a), (b), (c), 0, 0, 0)
#define SB() __builtin_amdgcn_sched_barrier(0)

#define ROTMODE 1

constexpr unsigned WS_E = 0;
constexpr unsigned WS_TMO = 256;
constexpr unsigned WS_F1 = 4096;
constexpr unsigned WS_F2 = WS_F1 + 32768;
constexpr unsigned WS_G = WS_F2 + 32768;
constexpr unsigned WS_P = WS_G + 8192;
constexpr unsigned PSZ = 4352;
constexpr unsigned WS_O = WS_P + 256 * PSZ;
constexpr unsigned WS_END = WS_O + 64 * 64 * 1024;
constexpr unsigned WS_STAMP = WS_END;

constexpr unsigned L_X = 0;
constexpr unsigned L_W = 49152;
constexpr unsigned L_WQ = 98304;
constexpr unsigned L_EX = 65536;
constexpr unsigned L_EXZ = 81920;
constexpr unsigned L_CTL = 86016;
constexpr unsigned L_KVZ = 90112;
constexpr unsigned L_F = 65536;
constexpr unsigned L_WOA = 0, L_WOB = 98304;
constexpr unsigned LDS_BYTES = 163840;
constexpr unsigned kSpinCap = 1u << 18;

#define STAMP_OUT(N) do { } while (0)
#define STAMP(n) do { } while (0)

DI unsigned pk2(float a, float b) { f32x2 v = {a, b}; return __builtin_bit_cast(unsigned, __builtin_convertvector(v, bf16x2_t)); }
DI float phi(float t) {
    float e = __builtin_amdgcn_fmed3f(__builtin_amdgcn_exp2f(t * 1.4426950408889634f), 0.f, 1.f);
    return (__builtin_fmaxf(t, 0.f) + 1e-6f) + e;
}
DI bf16x8 pack8(const f32x16& x, int s) {
    u32x4 p = {pk2(x[8 * s], x[8 * s + 1]), pk2(x[8 * s + 2], x[8 * s + 3]), pk2(x[8 * s + 4], x[8 * s + 5]), pk2(x[8 * s + 6], x[8 * s + 7])};
    return __builtin_bit_cast(bf16x8, p);
}
DI bf16x8 pack8phi(const f32x16& x, int s) {
    u32x4 p = {pk2(phi(x[8 * s]), phi(x[8 * s + 1])), pk2(phi(x[8 * s + 2]), phi(x[8 * s + 3])), pk2(phi(x[8 * s + 4]), phi(x[8 * s + 5])), pk2(phi(x[8 * s + 6]), phi(x[8 * s + 7]))};
    return __builtin_bit_cast(bf16x8, p);
}
DI bf16x8 lds_frag(ldsp p) { return *(const LAS bf16x8*)p; }
typedef short v4i16_t __attribute__((ext_vector_type(4)));
DI s16x4 vtr(ldsp p) { return __builtin_bit_cast(s16x4, __builtin_amdgcn_ds_read_tr16_b64_v4i16((LAS v4i16_t*)p)); }
DI bf16x8 tr_frag(ldsp p, int off0, int off1) {
    s16x4 lo = vtr(p + off0), hi = vtr(p + off1);
    return __builtin_shufflevector(lo, hi, 0, 1, 2, 3, 4, 5, 6, 7);
}
DI void glds(const void* sbase, unsigned voff, unsigned lds) {
    unsigned keep;
    asm volatile("s_mov_b32 %0, m0\n\ts_mov_b32 m0, %3\n\ts_nop 0\n\tglobal_load_lds_dwordx4 %1, %2\n\ts_mov_b32 m0, %0"
                 : "=&s"(keep) : "v"(voff), "s"(sbase), "s"(lds) : "memory");
}
#define WAITV_BAR(N) asm volatile("s_waitcnt vmcnt(" #N ")\n\ts_barrier" ::: "memory")
#define WAITL_BAR() asm volatile("s_waitcnt lgkmcnt(0)\n\ts_barrier" ::: "memory")
#define DRAIN() asm volatile("s_waitcnt vmcnt(0)" ::: "memory")

DI u64 read_epoch(const void* p) {
    u64 e;
    asm volatile("s_load_dwordx2 %0, %1, 0x0\n\ts_waitcnt lgkmcnt(0)" : "=s"(e) : "s"(p) : "memory");
    return e;
}
DI void poll16(GAS char* base, u64 tag, GAS char* ws, int lane) {
    gu64* f = (gu64*)(base + (lane & 15) * 128);
    for (unsigned spins = 0;; ++spins) {
        u64 v = __hip_atomic_load(f, RLX_AGENT);
        if (__all(v == tag)) break;
        if (spins > kSpinCap) { if (lane == 0) __hip_atomic_store((gu32*)(ws + WS_TMO), 1u, RLX_AGENT); break; }
        __builtin_amdgcn_s_sleep(1);
    }
    asm volatile("" ::: "memory");
}

__global__ __launch_bounds__(512, 2) void fused_kernel(const u16* __restrict__ x, const u16* __restrict__ wA, const u16* __restrict__ woA,
                                                       const u16* __restrict__ wB, const u16* __restrict__ woB, const u16* __restrict__ gw,
                                                       u16* __restrict__ out, char* wsp) {
    extern __shared__ __attribute__((aligned(16))) char smem[];
    GAS char* ws = (GAS char*)wsp;
    const ldsp lds = (ldsp)smem;
    const unsigned lds0 = (unsigned)(uintptr_t)lds;
    const int wave = __builtin_amdgcn_readfirstlane(threadIdx.x >> 6), lane = threadIdx.x & 63;
    const int b = blockIdx.x, xcd = b & 7, idx = b >> 3, il = idx & 7, jl = idx >> 3;
    const int ti = (xcd & 1) * 8 + il, tj = (xcd >> 1) * 4 + jl;
    const int rot = ROTMODE == 0 ? 0 : ((il + 2 * jl) & 7);
    const int t64 = xcd * 4 + (idx & 3), n64 = idx >> 2;
    const __amdgpu_buffer_rsrc_t rws = __builtin_amdgcn_make_buffer_rsrc(wsp, 0, WS_END, 0x00020000);
    STAMP(0);

    if (wave >= 4) {
        const int lw = wave - 4;
        const char* wbr = (const char*)((tj >> 3) ? wB : wA) + (tj & 7) * 128;
        const unsigned vx0 = (lane >> 3) * 1024 + (((lane & 7) ^ (lane >> 4)) << 4), vx1 = vx0 ^ 64;
        const unsigned vw = (lane >> 3) * 3072 + (((lane & 7) ^ (((lane >> 4) & 1) << 2)) << 4);
        const char* xs = (const char*)x + (size_t)(ti * 128 + lw * 32) * 1024;
        auto issue_kv = [&](int s) {
            const int cc = (s + rot) & 7, slot = (s + 2) % 3;
#pragma unroll
            for (int q = 0; q < 4; ++q) glds(xs + q * 8192 + cc * 128, (q & 1) ? vx1 : vx0, lds0 + L_X + slot * 16384 + lw * 4096 + q * 1024);
#pragma unroll
            for (int part = 0; part < 2; ++part)
#pragma unroll
                for (int q = 0; q < 2; ++q)
                    glds(wbr + (size_t)(cc * 64 + lw * 16 + q * 8) * 3072 + 1024 + part * 1024, vw, lds0 + L_W + slot * 16384 + part * 8192 + (lw * 2 + q) * 1024);
        };
        auto issue_q = [&](int s) {
            const int cc = (s + rot) & 7;
#pragma unroll
            for (int q = 0; q < 2; ++q) glds(wbr + (size_t)(cc * 64 + lw * 16 + q * 8) * 3072, vw, lds0 + L_WQ + s * 8192 + (lw * 2 + q) * 1024);
        };
        issue_kv(0); issue_kv(1);
        WAITV_BAR(8);
        STAMP(1);
        issue_kv(2);
        const u64 tag = read_epoch(wsp) + 1;
        WAITV_BAR(8);  issue_kv(3);
        WAITV_BAR(8);  issue_kv(4);
        WAITV_BAR(8);  issue_kv(5);
        WAITV_BAR(8);  issue_kv(6);
        WAITV_BAR(8);  issue_kv(7);
        WAITV_BAR(8);
        issue_q(0); issue_q(1); issue_q(2); issue_q(3);
        WAITV_BAR(8);
        STAMP(2);
        issue_q(4); issue_q(5); issue_q(6); issue_q(7);
        if (lw == 0 && lane < 2) *(LAS unsigned*)(lds + L_CTL + lane * 4) = 0u;
        asm volatile("s_waitcnt vmcnt(0) lgkmcnt(0)\n\ts_barrier" ::: "memory");
        STAMP(3);
        {
            f32x4 a = *(const LAS f32x4*)(lds + L_EX + lw * 1024 + lane * 16);
            float z = *(const LAS float*)(lds + L_EXZ + lw * 64 + (lane & 15) * 4);
#pragma unroll
            for (int w = 1; w < 4; ++w) {
                a += *(const LAS f32x4*)(lds + L_EX + w * 4096 + lw * 1024 + lane * 16);
                z += *(const LAS float*)(lds + L_EXZ + w * 256 + lw * 64 + (lane & 15) * 4);
            }
            const unsigned pb = WS_P + (tj * 16 + ti) * PSZ;
            asm volatile("" : "+v"(a), "+v"(z));
            STAMP(9);
            __builtin_amdgcn_raw_buffer_store_b128(__builtin_bit_cast(u32x4, a), rws, lane * 16, pb + lw * 1024, 16);
            if (lane < 16) __builtin_amdgcn_raw_buffer_store_b32(__builtin_bit_cast(unsigned, z), rws, lane * 4, pb + 4096 + lw * 64, 16);
            STAMP(10);
            DRAIN();
            STAMP(11);
            unsigned old = 0;
            if (lane == 0) old = __hip_atomic_fetch_add((LAS unsigned*)(lds + L_CTL), 1u, __ATOMIC_RELAXED, __HIP_MEMORY_SCOPE_WORKGROUP);
            old = __builtin_amdgcn_readfirstlane(old);
            if (old == 3 && lane == 0) __hip_atomic_store((gu64*)(ws + WS_F1 + (tj * 16 + ti) * 128), tag, RLX_AGENT);
        }
        STAMP(4);
        {
            const int tok = ti * 128 + tj * 8 + lw * 2;
            const u32x4 g4 = *(const u32x4*)((const char*)gw + lane * 16);
            const u32x4 x0 = *(const u32x4*)((const char*)x + (size_t)tok * 1024 + lane * 16);
            const u32x4 x1 = *(const u32x4*)((const char*)x + (size_t)(tok + 1) * 1024 + lane * 16);
            float s0 = 0.f, s1 = 0.f;
#pragma unroll
            for (int e = 0; e < 4; ++e) {
                const float gl = __uint_as_float(g4[e] << 16), gh = __uint_as_float(g4[e] & 0xffff0000u);
                s0 = __builtin_fmaf(__uint_as_float(x0[e] << 16), gl, s0); s0 = __builtin_fmaf(__uint_as_float(x0[e] & 0xffff0000u), gh, s0);
                s1 = __builtin_fmaf(__uint_as_float(x1[e] << 16), gl, s1); s1 = __builtin_fmaf(__uint_as_float(x1[e] & 0xffff0000u), gh, s1);
            }
#pragma unroll
            for (int m = 32; m >= 1; m >>= 1) { s0 += __shfl_xor(s0, m); s1 += __shfl_xor(s1, m); }
            const float g0 = 1.f / (1.f + __expf(-s0)), g1 = 1.f / (1.f + __expf(-s1));
            if (lane == 0) __hip_atomic_store((gu64*)(ws + WS_G + tok * 4), (u64)__float_as_uint(g0) | ((u64)__float_as_uint(g1) << 32), RLX_AGENT);
        }
        const unsigned vo = (lane >> 3) * 1024 + (((lane & 7) ^ (((lane >> 4) & 1) << 2)) << 4);
        {
            const char* src = (const char*)woA + n64 * 128;
#pragma unroll
            for (int u = 0; u < 16; ++u) { const int n = lw * 16 + ((u + 4 * (idx & 3)) & 15); glds(src + (size_t)n * 8192, vo, lds0 + L_WOA + n * 1024); }
        }
        STAMP(5);
        poll16(ws + WS_F1 + tj * 16 * 128, tag, ws, lane);
        STAMP(6);
        {
            const unsigned pb = WS_P + tj * 16 * PSZ;
            u32x4 v[16];
#pragma unroll
            for (int p = 0; p < 16; ++p) v[p] = __builtin_amdgcn_raw_buffer_load_b128(rws, lane * 16, pb + p * PSZ + lw * 1024, 16);
            const u32x4 zz = __builtin_amdgcn_raw_buffer_load_b128(rws, (lane >> 2) * PSZ + (lane & 3) * 16, pb + 4096 + lw * 64, 16);
            f32x4 a = __builtin_bit_cast(f32x4, v[0]);
#pragma unroll
            for (int p = 1; p < 16; ++p) a += __builtin_bit_cast(f32x4, v[p]);
            f32x4 z = __builtin_bit_cast(f32x4, zz);
#pragma unroll
            for (int e = 0; e < 4; ++e) {
                float t = z[e];
                t += __builtin_bit_cast(float, __builtin_amdgcn_update_dpp(0, __builtin_bit_cast(int, t), 0x124, 0xf, 0xf, true));
                t += __builtin_bit_cast(float, __builtin_amdgcn_update_dpp(0, __builtin_bit_cast(int, t), 0x128, 0xf, 0xf, true));
                auto r16 = __builtin_amdgcn_permlane16_swap(__float_as_uint(t), __float_as_uint(t), false, false);
                t = __uint_as_float(r16[0]) + __uint_as_float(r16[1]);
                auto r32 = __builtin_amdgcn_permlane32_swap(__float_as_uint(t), __float_as_uint(t), false, false);
                z[e] = __uint_as_float(r32[0]) + __uint_as_float(r32[1]);
            }
            *(LAS u32x2*)(lds + L_KVZ + lw * 1024 + (lane & 31) * 16 + (lane >> 5) * 8) = (u32x2){pk2(a[0], a[1]), pk2(a[2], a[3])};
            if (lane < 4) *(LAS u32x2*)(lds + L_KVZ + lw * 1024 + 512 + (lane & 1) * 16 + (lane >> 1) * 8) = (u32x2){pk2(z[0], z[1]), pk2(z[2], z[3])};
        }
        asm volatile("s_waitcnt vmcnt(0) lgkmcnt(0)\n\ts_barrier" ::: "memory");
        STAMP(7);
        {
            const char* src = (const char*)woB + n64 * 128;
#pragma unroll
            for (int u = 0; u < 16; ++u) { const int n = lw * 16 + ((u + 4 * (idx & 3)) & 15); glds(src + (size_t)n * 8192, vo, lds0 + L_WOB + n * 1024); }
        }
        WAITV_BAR(0);
        STAMP(8);
        STAMP_OUT(12);
        return;
    }

    const int w = wave, r = lane & 31, h = lane >> 5;
    const int q4 = (lane & 15) >> 2, p4 = lane & 3, ch = (lane >> 4) & 1, sw = (q4 >> 1) & 1;
    const u64 tag = read_epoch(wsp) + 1;
    ldsp xa[4];
    {
        const int row = 32 * w + r;
        const unsigned a0 = row * 128 + ((h ^ ((row >> 1) & 7)) << 4);
#pragma unroll
        for (int ks = 0; ks < 4; ++ks) xa[ks] = lds + L_X + (a0 ^ (ks << 5));
    }
    ldsp wb[2];
#pragma unroll
    for (int ct = 0; ct < 2; ++ct) wb[ct] = lds + L_W + (8 * h + q4) * 128 + ((ct ^ sw) * 64) + ch * 32 + p4 * 8;

    bf16x8 X[32];
    f32x16 aK[2], aV[2];
#pragma unroll
    for (int i = 0; i < 16; ++i) { aK[0][i] = 0.f; aK[1][i] = 0.f; aV[0][i] = 0.f; aV[1][i] = 0.f; }
    bf16x8 WK[2][2], WV[2][2];
#define OFFW(s, ks) ((((s) + 2) % 3) * 16384 + (ks) * 2048)
#define RD_K(buf, ct, s, ks) WK[buf][ct] = tr_frag(wb[ct], OFFW(s, ks), OFFW(s, ks) + 512)
#define RD_V(buf, ct, s, ks) WV[buf][ct] = tr_frag(wb[ct], OFFW(s, ks) + 8192, OFFW(s, ks) + 8192 + 512)
#define RD_X1(s, ks) X[4 * (s) + (ks)] = lds_frag(xa[ks] + (((s) + 2) % 3) * 16384)
    WAITL_BAR();
    STAMP(1);
    RD_X1(0, 0); RD_K(0, 0, 0, 0); RD_V(0, 0, 0, 0); RD_K(0, 1, 0, 0); RD_V(0, 1, 0, 0); RD_X1(0, 1); RD_X1(0, 2); RD_X1(0, 3);
    SB();
#pragma unroll
    for (int n = 0; n < 32; ++n) {
        const int s = n >> 2, ks = n & 3, cur = n & 1, nxt = cur ^ 1;
        const int s2 = (n + 1) >> 2, ks2 = (n + 1) & 3;
        if (ks == 3 && n < 31) { WAITL_BAR(); }
        aK[0] = MFMA(X[n], WK[cur][0], aK[0]); if (n < 31) { RD_K(nxt, 0, s2, ks2); if (ks == 3) RD_X1(s2, 0); } SB();
        aV[0] = MFMA(X[n], WV[cur][0], aV[0]); if (n < 31) { RD_V(nxt, 0, s2, ks2); if (ks == 3) RD_X1(s2, 1); } SB();
        aK[1] = MFMA(X[n], WK[cur][1], aK[1]); if (n < 31) { RD_K(nxt, 1, s2, ks2); if (ks == 3) RD_X1(s2, 2); } SB();
        aV[1] = MFMA(X[n], WV[cur][1], aV[1]); if (n < 31) { RD_V(nxt, 1, s2, ks2); if (ks == 3) RD_X1(s2, 3); } SB();
        (void)s;
    }
    STAMP(2);
    {
        const bf16x8 ones = {0x3F80, 0x3F80, 0x3F80, 0x3F80, 0x3F80, 0x3F80, 0x3F80, 0x3F80};
#pragma unroll
        for (int hp = 0; hp < 2; ++hp) {
            f32x16 ckv, cz;
#pragma unroll
            for (int i = 0; i < 16; ++i) { ckv[i] = 0.f; cz[i] = 0.f; }
#pragma unroll
            for (int ks = 0; ks < 2; ++ks) {
                const bf16x8 pk = pack8phi(aK[hp], ks), vv = pack8(aV[hp], ks);
                ckv = MFMA(pk, vv, ckv);
                cz = MFMA(ones, pk, cz);
            }
            const int s = (lane >> 4) & 1;
            ldsp dst = lds + L_EX + w * 4096 + (2 * hp + s) * 1024 + (h * 16 + (lane & 15)) * 16;
            if (s == 0) {
                *(LAS f32x4*)(dst) = (f32x4){ckv[0], ckv[1], ckv[2], ckv[3]}; *(LAS f32x4*)(dst + 512) = (f32x4){ckv[4], ckv[5], ckv[6], ckv[7]};
            } else {
                *(LAS f32x4*)(dst) = (f32x4){ckv[8], ckv[9], ckv[10], ckv[11]}; *(LAS f32x4*)(dst + 512) = (f32x4){ckv[12], ckv[13], ckv[14], ckv[15]};
            }
            if (lane < 32) *(LAS float*)(lds + L_EXZ + w * 256 + hp * 128 + lane * 4) = cz[0];
        }
    }
    WAITL_BAR();
    STAMP(3);
    f32x16 aQ[2];
#pragma unroll
    for (int i = 0; i < 16; ++i) { aQ[0][i] = 0.f; aQ[1][i] = 0.f; }
    {
        ldsp wq0 = wb[0] + (L_WQ - L_W), wq1 = wb[1] + (L_WQ - L_W);
        asm volatile("" : "+v"(wq0), "+v"(wq1));
        bf16x8 WQ[2][2];
#define RD_Q1(buf, ct, n) WQ[buf][ct] = tr_frag((ct) ? wq1 : wq0, ((n) >> 2) * 8192 + ((n) & 3) * 2048, ((n) >> 2) * 8192 + ((n) & 3) * 2048 + 512)
        RD_Q1(0, 0, 0); RD_Q1(0, 1, 0); SB();
#pragma unroll
        for (int n = 0; n < 32; ++n) {
            aQ[0] = MFMA(WQ[n & 1][0], X[n], aQ[0]); if (n < 31) RD_Q1((n + 1) & 1, 0, n + 1); SB();
            aQ[1] = MFMA(WQ[n & 1][1], X[n], aQ[1]); if (n < 31) RD_Q1((n + 1) & 1, 1, n + 1); SB();
        }
    }
    STAMP(4);
    bf16x8 PQ[4];
#pragma unroll
    for (int hd = 0; hd < 4; ++hd) PQ[hd] = pack8phi(aQ[hd >> 1], hd & 1);
    STAMP(5);
    WAITL_BAR();
    STAMP(6);
    {
        ldsp ka = lds + L_KVZ + (r < 16 ? (h * 16 + r) * 16 : 512 + h * 16);
        f32x16 zero;
#pragma unroll
        for (int i = 0; i < 16; ++i) zero[i] = 0.f;
#pragma unroll
        for (int hd = 0; hd < 4; ++hd) {
            const bf16x8 A = lds_frag(ka + hd * 1024);
            const f32x16 c = MFMA(A, PQ[hd], zero);
            const float inv = __builtin_amdgcn_rcpf(c[8] + 1e-6f);
            const u32x4 o = {pk2(c[0] * inv, c[1] * inv), pk2(c[2] * inv, c[3] * inv), pk2(c[4] * inv, c[5] * inv), pk2(c[6] * inv, c[7] * inv)};
            __builtin_amdgcn_raw_buffer_store_b128(o, rws, lane * 16, WS_O + ((ti * 4 + w) * 64 + tj * 4 + hd) * 1024, 16);
        }
        DRAIN();
        unsigned old = 0;
        if (lane == 0) old = __hip_atomic_fetch_add((LAS unsigned*)(lds + L_CTL + 4), 1u, __ATOMIC_RELAXED, __HIP_MEMORY_SCOPE_WORKGROUP);
        old = __builtin_amdgcn_readfirstlane(old);
        if (old == 3 && lane == 0) __hip_atomic_store((gu64*)(ws + WS_F2 + (ti * 16 + tj) * 128), tag, RLX_AGENT);
    }
    STAMP(7);
    const int th = w & 1, kh = w >> 1;
    poll16(ws + WS_F2 + (t64 >> 1) * 16 * 128, tag, ws, lane);
    STAMP(8);
    if (b == 0 && w == 0 && lane == 0) __hip_atomic_store((gu64*)(ws + WS_E), tag, RLX_AGENT);
    u32x4 O[32];
    {
        const unsigned ob = WS_O + ((t64 * 2 + th) * 64 + kh * 32) * 1024;
#pragma unroll
        for (int n = 0; n < 32; ++n) O[n] = __builtin_amdgcn_raw_buffer_load_b128(rws, lane * 16, ob + n * 1024, 16);
    }
    const float g = __uint_as_float(__builtin_amdgcn_raw_buffer_load_b32(rws, r * 4, WS_G + (t64 * 64 + th * 32) * 4, 16));
    SB();
    WAITL_BAR();
    STAMP(9);
    f32x16 acc[2];
#pragma unroll
    for (int i = 0; i < 16; ++i) { acc[0][i] = 0.f; acc[1][i] = 0.f; }
    {
        ldsp ob[2];
#pragma unroll
        for (int nt = 0; nt < 2; ++nt) ob[nt] = lds + (kh ? L_WOB : L_WOA) + (4 * h + q4) * 128 + ((nt ^ sw) * 64) + ch * 32 + p4 * 8;
        asm volatile("" : "+v"(ob[0]), "+v"(ob[1]));
        bf16x8 WO[2][2];
#define RD_O1(buf, nt, n) WO[buf][nt] = tr_frag(ob[nt], (n) * 2048, (n) * 2048 + 1024)
        RD_O1(0, 0, 0); RD_O1(0, 1, 0); SB();
#pragma unroll
        for (int n = 0; n < 32; ++n) {
            const bf16x8 of = __builtin_bit_cast(bf16x8, O[n]);
            acc[0] = MFMA(WO[n & 1][0], of, acc[0]); if (n < 31) RD_O1((n + 1) & 1, 0, n + 1); SB();
            acc[1] = MFMA(WO[n & 1][1], of, acc[1]); if (n < 31) RD_O1((n + 1) & 1, 1, n + 1); SB();
        }
    }
    STAMP(10);
    {
        const float f = kh ? 1.f - g : g;
        const int tk = 32 * th + r;
#pragma unroll
        for (int nt = 0; nt < 2; ++nt)
#pragma unroll
            for (int q = 0; q < 4; ++q) {
                const int pc = 8 * nt + 2 * q + h;
                *(LAS f32x4*)(lds + L_F + kh * 16384 + tk * 256 + ((pc ^ (tk & 15)) << 4)) =
                    (f32x4){acc[nt][4 * q] * f, acc[nt][4 * q + 1] * f, acc[nt][4 * q + 2] * f, acc[nt][4 * q + 3] * f};
            }
        WAITL_BAR();
#pragma unroll
        for (int u = 0; u < 2; ++u) {
            const int tk2 = 16 * w + 8 * u + (lane >> 3), p8 = lane & 7;
            ldsp rowp = lds + L_F + tk2 * 256;
            const int c0 = ((2 * p8) ^ (tk2 & 15)) << 4, c1 = ((2 * p8 + 1) ^ (tk2 & 15)) << 4;
            const f32x4 a0 = *(const LAS f32x4*)(rowp + c0), a1 = *(const LAS f32x4*)(rowp + c1);
            const f32x4 b0 = *(const LAS f32x4*)(rowp + 16384 + c0), b1 = *(const LAS f32x4*)(rowp + 16384 + c1);
            const f32x4 s0 = a0 + b0, s1 = a1 + b1;
            const u32x4 o = {pk2(s0[0], s0[1]), pk2(s0[2], s0[3]), pk2(s1[0], s1[1]), pk2(s1[2], s1[3])};
            *(u32x4*)((char*)out + (size_t)(t64 * 64 + tk2) * 1024 + n64 * 128 + p8 * 16) = o;
        }
    }
    STAMP(11);
    STAMP_OUT(12);
}

extern "C" void kernel_launch(void* const* d_in, const int* in_sizes, int n_in, void* d_out, int out_size, void* d_ws, size_t ws_size,
                              hipStream_t stream) {
    (void)in_sizes; (void)n_in; (void)out_size; (void)ws_size;
    (void)hipFuncSetAttribute((const void*)fused_kernel, hipFuncAttributeMaxDynamicSharedMemorySize, LDS_BYTES);
    fused_kernel<<<256, 512, LDS_BYTES, stream>>>((const u16*)d_in[0], (const u16*)d_in[1], (const u16*)d_in[2], (const u16*)d_in[4],
                                                  (const u16*)d_in[5], (const u16*)d_in[6], (u16*)d_out, (char*)d_ws);
}
```

```cpp
#include <hip/hip_runtime.h>
#include <hip/hip_bf16.h>
#include <stdint.h>

typedef unsigned short u16;
typedef unsigned long long u64;
typedef short bf16x8 __attribute__((ext_vector_type(8)));
typedef short s16x4 __attribute__((ext_vector_type(4)));
typedef float f32x16 __attribute__((ext_vector_type(16)));
typedef float f32x4 __attribute__((ext_vector_type(4)));
typedef float f32x2 __attribute__((ext_vector_type(2)));
typedef unsigned u32x4 __attribute__((ext_vector_type(4)));
typedef unsigned u32x2 __attribute__((ext_vector_type(2)));
typedef __bf16 bf16x2_t __attribute__((ext_vector_type(2)));
#define GAS __attribute__((address_space(1)))
#define LAS __attribute__((address_space(3)))
typedef GAS u64 gu64;
typedef GAS unsigned gu32;
typedef LAS char* ldsp;
#define DI __device__ __forceinline__
#define RLX_AGENT __ATOMIC_RELAXED, __HIP_MEMORY_SCOPE_AGENT
#define MFMA(a, b, c) __builtin_amdgcn_mfma_f32_32x32x16_bf16((a), (b), (c), 0, 0, 0)
#define SB() __builtin_amdgcn_sched_barrier(0)

#define ROTMODE 1

constexpr unsigned WS_E = 0;
constexpr unsigned WS_TMO = 256;
constexpr unsigned WS_F1 = 4096;
constexpr unsigned WS_F2 = WS_F1 + 32768;
constexpr unsigned WS_G = WS_F2 + 32768;
constexpr unsigned WS_P = WS_G + 8192;
constexpr unsigned PSZ = 4352;
constexpr unsigned WS_O = WS_P + 256 * PSZ;
constexpr unsigned WS_END = WS_O + 64 * 64 * 1024;
constexpr unsigned WS_STAMP = WS_END;

constexpr unsigned L_X = 0;
constexpr unsigned L_W = 49152;
constexpr unsigned L_WQ = 98304;
constexpr unsigned L_EX = 65536;
constexpr unsigned L_EXZ = 81920;
constexpr unsigned L_CTL = 86016;
constexpr unsigned L_KVZ = 90112;
constexpr unsigned L_F = 65536;
constexpr unsigned L_WOA = 0, L_WOB = 98304;
constexpr unsigned LDS_BYTES = 163840;
constexpr unsigned kSpinCap = 1u << 18;

#define STAMP_OUT(N) do { } while (0)
#define STAMP(n) do { } while (0)

DI unsigned pk2(float a, float b) { f32x2 v = {a, b}; return __builtin_bit_cast(unsigned, __builtin_convertvector(v, bf16x2_t)); }
DI float phi(float t) {
    float e = __builtin_amdgcn_fmed3f(__builtin_amdgcn_exp2f(t * 1.4426950408889634f), 0.f, 1.f);
    return (__builtin_fmaxf(t, 0.f) + 1e-6f) + e;
}
DI bf16x8 pack8(const f32x16& x, int s) {
    u32x4 p = {pk2(x[8 * s], x[8 * s + 1]), pk2(x[8 * s + 2], x[8 * s + 3]), pk2(x[8 * s + 4], x[8 * s + 5]), pk2(x[8 * s + 6], x[8 * s + 7])};
    return __builtin_bit_cast(bf16x8, p);
}
DI bf16x8 pack8phi(const f32x16& x, int s) {
    u32x4 p = {pk2(phi(x[8 * s]), phi(x[8 * s + 1])), pk2(phi(x[8 * s + 2]), phi(x[8 * s + 3])), pk2(phi(x[8 * s + 4]), phi(x[8 * s + 5])), pk2(phi(x[8 * s + 6]), phi(x[8 * s + 7]))};
    return __builtin_bit_cast(bf16x8, p);
}
DI bf16x8 lds_frag(ldsp p) { return *(const LAS bf16x8*)p; }
typedef short v4i16_t __attribute__((ext_vector_type(4)));
DI s16x4 vtr(ldsp p) { return __builtin_bit_cast(s16x4, __builtin_amdgcn_ds_read_tr16_b64_v4i16((LAS v4i16_t*)p)); }
DI bf16x8 tr_frag(ldsp p, int off0, int off1) {
    s16x4 lo = vtr(p + off0), hi = vtr(p + off1);
    return __builtin_shufflevector(lo, hi, 0, 1, 2, 3, 4, 5, 6, 7);
}
DI void glds(const void* sbase, unsigned voff, unsigned lds) {
    unsigned keep;
    asm volatile("s_mov_b32 %0, m0\n\ts_mov_b32 m0, %3\n\ts_nop 0\n\tglobal_load_lds_dwordx4 %1, %2\n\ts_mov_b32 m0, %0"
                 : "=&s"(keep) : "v"(voff), "s"(sbase), "s"(lds) : "memory");
}
#define WAITV_BAR(N) asm volatile("s_waitcnt vmcnt(" #N ")\n\ts_barrier" ::: "memory")
#define WAITL_BAR() asm volatile("s_waitcnt lgkmcnt(0)\n\ts_barrier" ::: "memory")
#define DRAIN() asm volatile("s_waitcnt vmcnt(0)" ::: "memory")

DI u64 read_epoch(const void* p) {
    u64 e;
    asm volatile("s_load_dwordx2 %0, %1, 0x0\n\ts_waitcnt lgkmcnt(0)" : "=s"(e) : "s"(p) : "memory");
    return e;
}
DI void poll16(GAS char* base, u64 tag, GAS char* ws, int lane) {
    gu64* f = (gu64*)(base + (lane & 15) * 128);
    for (unsigned spins = 0;; ++spins) {
        u64 v = __hip_atomic_load(f, RLX_AGENT);
        if (__all(v == tag)) break;
        if (spins > kSpinCap) { if (lane == 0) __hip_atomic_store((gu32*)(ws + WS_TMO), 1u, RLX_AGENT); break; }
        __builtin_amdgcn_s_sleep(1);
    }
    asm volatile("" ::: "memory");
}

__global__ __launch_bounds__(512, 2) void fused_kernel(const u16* __restrict__ x, const u16* __restrict__ wA, const u16* __restrict__ woA,
                                                       const u16* __restrict__ wB, const u16* __restrict__ woB, const u16* __restrict__ gw,
                                                       u16* __restrict__ out, char* wsp) {
    extern __shared__ __attribute__((aligned(16))) char smem[];
    GAS char* ws = (GAS char*)wsp;
    const ldsp lds = (ldsp)smem;
    const unsigned lds0 = (unsigned)(uintptr_t)lds;
    const int wave = __builtin_amdgcn_readfirstlane(threadIdx.x >> 6), lane = threadIdx.x & 63;
    const int b = blockIdx.x, xcd = b & 7, idx = b >> 3, il = idx & 7, jl = idx >> 3;
    const int ti = (xcd & 1) * 8 + il, tj = (xcd >> 1) * 4 + jl;
    const int rot = ROTMODE == 0 ? 0 : ((il + 2 * jl) & 7);
    const int t64 = xcd * 4 + (idx & 3), n64 = idx >> 2;
    const __amdgpu_buffer_rsrc_t rws = __builtin_amdgcn_make_buffer_rsrc(wsp, 0, WS_END, 0x00020000);
    const u64 tpad0 = __builtin_amdgcn_s_memrealtime();
    STAMP(0);

    if (wave >= 4) {
        const int lw = wave - 4;
        const char* wbr = (const char*)((tj >> 3) ? wB : wA) + (tj & 7) * 128;
        const unsigned vx0 = (lane >> 3) * 1024 + (((lane & 7) ^ (lane >> 4)) << 4), vx1 = vx0 ^ 64;
        const unsigned vw = (lane >> 3) * 3072 + (((lane & 7) ^ (((lane >> 4) & 1) << 2)) << 4);
        const char* xs = (const char*)x + (size_t)(ti * 128 + lw * 32) * 1024;
        auto issue_kv = [&](int s) {
            const int cc = (s + rot) & 7, slot = (s + 2) % 3;
#pragma unroll
            for (int q = 0; q < 4; ++q) glds(xs + q * 8192 + cc * 128, (q & 1) ? vx1 : vx0, lds0 + L_X + slot * 16384 + lw * 4096 + q * 1024);
#pragma unroll
            for (int part = 0; part < 2; ++part)
#pragma unroll
                for (int q = 0; q < 2; ++q)
                    glds(wbr + (size_t)(cc * 64 + lw * 16 + q * 8) * 3072 + 1024 + part * 1024, vw, lds0 + L_W + slot * 16384 + part * 8192 + (lw * 2 + q) * 1024);
        };
        auto issue_q = [&](int s) {
            const int cc = (s + rot) & 7;
#pragma unroll
            for (int q = 0; q < 2; ++q) glds(wbr + (size_t)(cc * 64 + lw * 16 + q * 8) * 3072, vw, lds0 + L_WQ + s * 8192 + (lw * 2 + q) * 1024);
        };
        issue_kv(0); issue_kv(1);
        WAITV_BAR(8);
        STAMP(1);
        issue_kv(2);
        const u64 tag = read_epoch(wsp) + 1;
        WAITV_BAR(8);  issue_kv(3);
        WAITV_BAR(8);  issue_kv(4);
        WAITV_BAR(8);  issue_kv(5);
        WAITV_BAR(8);  issue_kv(6);
        WAITV_BAR(8);  issue_kv(7);
        WAITV_BAR(8);
        issue_q(0); issue_q(1); issue_q(2); issue_q(3);
        WAITV_BAR(8);
        STAMP(2);
        issue_q(4); issue_q(5); issue_q(6); issue_q(7);
        if (lw == 0 && lane < 2) *(LAS unsigned*)(lds + L_CTL + lane * 4) = 0u;
        asm volatile("s_waitcnt vmcnt(0) lgkmcnt(0)\n\ts_barrier" ::: "memory");
        STAMP(3);
        {
            f32x4 a = *(const LAS f32x4*)(lds + L_EX + lw * 1024 + lane * 16);
            float z = *(const LAS float*)(lds + L_EXZ + lw * 64 + (lane & 15) * 4);
#pragma unroll
            for (int w = 1; w < 4; ++w) {
                a += *(const LAS f32x4*)(lds + L_EX + w * 4096 + lw * 1024 + lane * 16);
                z += *(const LAS float*)(lds + L_EXZ + w * 256 + lw * 64 + (lane & 15) * 4);
            }
            const unsigned pb = WS_P + (tj * 16 + ti) * PSZ;
            asm volatile("" : "+v"(a), "+v"(z));
            STAMP(9);
            __builtin_amdgcn_raw_buffer_store_b128(__builtin_bit_cast(u32x4, a), rws, lane * 16, pb + lw * 1024, 16);
            if (lane < 16) __builtin_amdgcn_raw_buffer_store_b32(__builtin_bit_cast(unsigned, z), rws, lane * 4, pb + 4096 + lw * 64, 16);
            STAMP(10);
    const __amdgpu_buffer_rsrc_t rout = __builtin_amdgcn_make_buffer_rsrc(out, 0, 2048 * 1024, 0x00020000);
            DRAIN();
            STAMP(11);
            unsigned old = 0;
            if (lane == 0) old = __hip_atomic_fetch_add((LAS unsigned*)(lds + L_CTL), 1u, __ATOMIC_RELAXED, __HIP_MEMORY_SCOPE_WORKGROUP);
            old = __builtin_amdgcn_readfirstlane(old);
            if (old == 3 && lane == 0) __hip_atomic_store((gu64*)(ws + WS_F1 + (tj * 16 + ti) * 128), tag, RLX_AGENT);
        }
        STAMP(4);
        {
            const int tok = ti * 128 + tj * 8 + lw * 2;
            const u32x4 g4 = *(const u32x4*)((const char*)gw + lane * 16);
            const u32x4 x0 = *(const u32x4*)((const char*)x + (size_t)tok * 1024 + lane * 16);
            const u32x4 x1 = *(const u32x4*)((const char*)x + (size_t)(tok + 1) * 1024 + lane * 16);
            float s0 = 0.f, s1 = 0.f;
#pragma unroll
            for (int e = 0; e < 4; ++e) {
                const float gl = __uint_as_float(g4[e] << 16), gh = __uint_as_float(g4[e] & 0xffff0000u);
                s0 = __builtin_fmaf(__uint_as_float(x0[e] << 16), gl, s0); s0 = __builtin_fmaf(__uint_as_float(x0[e] & 0xffff0000u), gh, s0);
                s1 = __builtin_fmaf(__uint_as_float(x1[e] << 16), gl, s1); s1 = __builtin_fmaf(__uint_as_float(x1[e] & 0xffff0000u), gh, s1);
            }
#pragma unroll
            for (int m = 32; m >= 1; m >>= 1) { s0 += __shfl_xor(s0, m); s1 += __shfl_xor(s1, m); }
            const float g0 = 1.f / (1.f + __expf(-s0)), g1 = 1.f / (1.f + __expf(-s1));
            if (lane == 0) __hip_atomic_store((gu64*)(ws + WS_G + tok * 4), (u64)__float_as_uint(g0) | ((u64)__float_as_uint(g1) << 32), RLX_AGENT);
        }
        const unsigned vo = (lane >> 3) * 1024 + (((lane & 7) ^ (((lane >> 4) & 1) << 2)) << 4);
        {
            const char* src = (const char*)woA + n64 * 128;
#pragma unroll
            for (int u = 0; u < 16; ++u) { const int n = lw * 16 + ((u + 4 * (idx & 3)) & 15); glds(src + (size_t)n * 8192, vo, lds0 + L_WOA + n * 1024); }
        }
        STAMP(5);
        poll16(ws + WS_F1 + tj * 16 * 128, tag, ws, lane);
        STAMP(6);
        {
            const unsigned pb = WS_P + tj * 16 * PSZ;
            u32x4 v[16];
#pragma unroll
            for (int p = 0; p < 16; ++p) v[p] = __builtin_amdgcn_raw_buffer_load_b128(rws, lane * 16, pb + p * PSZ + lw * 1024, 16);
            const u32x4 zz = __builtin_amdgcn_raw_buffer_load_b128(rws, (lane >> 2) * PSZ + (lane & 3) * 16, pb + 4096 + lw * 64, 16);
            f32x4 a = __builtin_bit_cast(f32x4, v[0]);
#pragma unroll
            for (int p = 1; p < 16; ++p) a += __builtin_bit_cast(f32x4, v[p]);
            f32x4 z = __builtin_bit_cast(f32x4, zz);
#pragma unroll
            for (int e = 0; e < 4; ++e) {
                float t = z[e];
                t += __builtin_bit_cast(float, __builtin_amdgcn_update_dpp(0, __builtin_bit_cast(int, t), 0x124, 0xf, 0xf, true));
                t += __builtin_bit_cast(float, __builtin_amdgcn_update_dpp(0, __builtin_bit_cast(int, t), 0x128, 0xf, 0xf, true));
                auto r16 = __builtin_amdgcn_permlane16_swap(__float_as_uint(t), __float_as_uint(t), false, false);
                t = __uint_as_float(r16[0]) + __uint_as_float(r16[1]);
                auto r32 = __builtin_amdgcn_permlane32_swap(__float_as_uint(t), __float_as_uint(t), false, false);
                z[e] = __uint_as_float(r32[0]) + __uint_as_float(r32[1]);
            }
            *(LAS u32x2*)(lds + L_KVZ + lw * 1024 + (lane & 31) * 16 + (lane >> 5) * 8) = (u32x2){pk2(a[0], a[1]), pk2(a[2], a[3])};
            if (lane < 4) *(LAS u32x2*)(lds + L_KVZ + lw * 1024 + 512 + (lane & 1) * 16 + (lane >> 1) * 8) = (u32x2){pk2(z[0], z[1]), pk2(z[2], z[3])};
        }
        asm volatile("s_waitcnt vmcnt(0) lgkmcnt(0)\n\ts_barrier" ::: "memory");
        STAMP(7);
        {
            const char* src = (const char*)woB + n64 * 128;
#pragma unroll
            for (int u = 0; u < 16; ++u) { const int n = lw * 16 + ((u + 4 * (idx & 3)) & 15); glds(src + (size_t)n * 8192, vo, lds0 + L_WOB + n * 1024); }
        }
        WAITV_BAR(0);
        STAMP(8);
        STAMP_OUT(12);
        return;
    }

    const int w = wave, r = lane & 31, h = lane >> 5;
    const int q4 = (lane & 15) >> 2, p4 = lane & 3, ch = (lane >> 4) & 1, sw = (q4 >> 1) & 1;
    const u64 tag = read_epoch(wsp) + 1;
    ldsp xa[4];
    {
        const int row = 32 * w + r;
        const unsigned a0 = row * 128 + ((h ^ ((row >> 1) & 7)) << 4);
#pragma unroll
        for (int ks = 0; ks < 4; ++ks) xa[ks] = lds + L_X + (a0 ^ (ks << 5));
    }
    ldsp wb[2];
#pragma unroll
    for (int ct = 0; ct < 2; ++ct) wb[ct] = lds + L_W + (8 * h + q4) * 128 + ((ct ^ sw) * 64) + ch * 32 + p4 * 8;

    bf16x8 X[32];
    f32x16 aK[2], aV[2];
#pragma unroll
    for (int i = 0; i < 16; ++i) { aK[0][i] = 0.f; aK[1][i] = 0.f; aV[0][i] = 0.f; aV[1][i] = 0.f; }
    bf16x8 WK[2][2], WV[2][2];
#define OFFW(s, ks) ((((s) + 2) % 3) * 16384 + (ks) * 2048)
#define RD_K(buf, ct, s, ks) WK[buf][ct] = tr_frag(wb[ct], OFFW(s, ks), OFFW(s, ks) + 512)
#define RD_V(buf, ct, s, ks) WV[buf][ct] = tr_frag(wb[ct], OFFW(s, ks) + 8192, OFFW(s, ks) + 8192 + 512)
#define RD_X1(s, ks) X[4 * (s) + (ks)] = lds_frag(xa[ks] + (((s) + 2) % 3) * 16384)
    WAITL_BAR();
    STAMP(1);
    RD_X1(0, 0); RD_K(0, 0, 0, 0); RD_V(0, 0, 0, 0); RD_K(0, 1, 0, 0); RD_V(0, 1, 0, 0); RD_X1(0, 1); RD_X1(0, 2); RD_X1(0, 3);
    SB();
#pragma unroll
    for (int n = 0; n < 32; ++n) {
        const int s = n >> 2, ks = n & 3, cur = n & 1, nxt = cur ^ 1;
        const int s2 = (n + 1) >> 2, ks2 = (n + 1) & 3;
        if (ks == 3 && n < 31) { WAITL_BAR(); }
        aK[0] = MFMA(X[n], WK[cur][0], aK[0]); if (n < 31) { RD_K(nxt, 0, s2, ks2); if (ks == 3) RD_X1(s2, 0); } SB();
        aV[0] = MFMA(X[n], WV[cur][0], aV[0]); if (n < 31) { RD_V(nxt, 0, s2, ks2); if (ks == 3) RD_X1(s2, 1); } SB();
        aK[1] = MFMA(X[n], WK[cur][1], aK[1]); if (n < 31) { RD_K(nxt, 1, s2, ks2); if (ks == 3) RD_X1(s2, 2); } SB();
        aV[1] = MFMA(X[n], WV[cur][1], aV[1]); if (n < 31) { RD_V(nxt, 1, s2, ks2); if (ks == 3) RD_X1(s2, 3); } SB();
        (void)s;
    }
    STAMP(2);
    {
        const bf16x8 ones = {0x3F80, 0x3F80, 0x3F80, 0x3F80, 0x3F80, 0x3F80, 0x3F80, 0x3F80};
#pragma unroll
        for (int hp = 0; hp < 2; ++hp) {
            f32x16 ckv, cz;
#pragma unroll
            for (int i = 0; i < 16; ++i) { ckv[i] = 0.f; cz[i] = 0.f; }
#pragma unroll
            for (int ks = 0; ks < 2; ++ks) {
                const bf16x8 pk = pack8phi(aK[hp], ks), vv = pack8(aV[hp], ks);
                ckv = MFMA(pk, vv, ckv);
                cz = MFMA(ones, pk, cz);
            }
            const int s = (lane >> 4) & 1;
            ldsp dst = lds + L_EX + w * 4096 + (2 * hp + s) * 1024 + (h * 16 + (lane & 15)) * 16;
            if (s == 0) {
                *(LAS f32x4*)(dst) = (f32x4){ckv[0], ckv[1], ckv[2], ckv[3]}; *(LAS f32x4*)(dst + 512) = (f32x4){ckv[4], ckv[5], ckv[6], ckv[7]};
            } else {
                *(LAS f32x4*)(dst) = (f32x4){ckv[8], ckv[9], ckv[10], ckv[11]}; *(LAS f32x4*)(dst + 512) = (f32x4){ckv[12], ckv[13], ckv[14], ckv[15]};
            }
            if (lane < 32) *(LAS float*)(lds + L_EXZ + w * 256 + hp * 128 + lane * 4) = cz[0];
        }
    }
    WAITL_BAR();
    STAMP(3);
    f32x16 aQ[2];
#pragma unroll
    for (int i = 0; i < 16; ++i) { aQ[0][i] = 0.f; aQ[1][i] = 0.f; }
    {
        ldsp wq0 = wb[0] + (L_WQ - L_W), wq1 = wb[1] + (L_WQ - L_W);
        asm volatile("" : "+v"(wq0), "+v"(wq1));
        bf16x8 WQ[2][2];
#define RD_Q1(buf, ct, n) WQ[buf][ct] = tr_frag((ct) ? wq1 : wq0, ((n) >> 2) * 8192 + ((n) & 3) * 2048, ((n) >> 2) * 8192 + ((n) & 3) * 2048 + 512)
        RD_Q1(0, 0, 0); RD_Q1(0, 1, 0); SB();
#pragma unroll
        for (int n = 0; n < 32; ++n) {
            aQ[0] = MFMA(WQ[n & 1][0], X[n], aQ[0]); if (n < 31) RD_Q1((n + 1) & 1, 0, n + 1); SB();
            aQ[1] = MFMA(WQ[n & 1][1], X[n], aQ[1]); if (n < 31) RD_Q1((n + 1) & 1, 1, n + 1); SB();
        }
    }
    STAMP(4);
    bf16x8 PQ[4];
#pragma unroll
    for (int hd = 0; hd < 4; ++hd) PQ[hd] = pack8phi(aQ[hd >> 1], hd & 1);
    STAMP(5);
    WAITL_BAR();
    STAMP(6);
    {
        ldsp ka = lds + L_KVZ + (r < 16 ? (h * 16 + r) * 16 : 512 + h * 16);
        f32x16 zero;
#pragma unroll
        for (int i = 0; i < 16; ++i) zero[i] = 0.f;
#pragma unroll
        for (int hd = 0; hd < 4; ++hd) {
            const bf16x8 A = lds_frag(ka + hd * 1024);
            const f32x16 c = MFMA(A, PQ[hd], zero);
            const float inv = __builtin_amdgcn_rcpf(c[8] + 1e-6f);
            const u32x4 o = {pk2(c[0] * inv, c[1] * inv), pk2(c[2] * inv, c[3] * inv), pk2(c[4] * inv, c[5] * inv), pk2(c[6] * inv, c[7] * inv)};
            __builtin_amdgcn_raw_buffer_store_b128(o, rws, lane * 16, WS_O + ((ti * 4 + w) * 64 + tj * 4 + hd) * 1024, 16);
        }
        DRAIN();
        unsigned old = 0;
        if (lane == 0) old = __hip_atomic_fetch_add((LAS unsigned*)(lds + L_CTL + 4), 1u, __ATOMIC_RELAXED, __HIP_MEMORY_SCOPE_WORKGROUP);
        old = __builtin_amdgcn_readfirstlane(old);
        if (old == 3 && lane == 0) __hip_atomic_store((gu64*)(ws + WS_F2 + (ti * 16 + tj) * 128), tag, RLX_AGENT);
    }
    STAMP(7);
    const int th = w & 1, kh = w >> 1;
    poll16(ws + WS_F2 + (t64 >> 1) * 16 * 128, tag, ws, lane);
    STAMP(8);
    if (b == 0 && w == 0 && lane == 0) __hip_atomic_store((gu64*)(ws + WS_E), tag, RLX_AGENT);
    u32x4 O[32];
    {
        const unsigned ob = WS_O + ((t64 * 2 + th) * 64 + kh * 32) * 1024;
#pragma unroll
        for (int n = 0; n < 32; ++n) O[n] = __builtin_amdgcn_raw_buffer_load_b128(rws, lane * 16, ob + n * 1024, 16);
    }
    const float g = __uint_as_float(__builtin_amdgcn_raw_buffer_load_b32(rws, r * 4, WS_G + (t64 * 64 + th * 32) * 4, 16));
    SB();
    WAITL_BAR();
    STAMP(9);
    f32x16 acc[2];
#pragma unroll
    for (int i = 0; i < 16; ++i) { acc[0][i] = 0.f; acc[1][i] = 0.f; }
    {
        ldsp ob[2];
#pragma unroll
        for (int nt = 0; nt < 2; ++nt) ob[nt] = lds + (kh ? L_WOB : L_WOA) + (4 * h + q4) * 128 + ((nt ^ sw) * 64) + ch * 32 + p4 * 8;
        asm volatile("" : "+v"(ob[0]), "+v"(ob[1]));
        bf16x8 WO[2][2];
#define RD_O1(buf, nt, n) WO[buf][nt] = tr_frag(ob[nt], (n) * 2048, (n) * 2048 + 1024)
        RD_O1(0, 0, 0); RD_O1(0, 1, 0); SB();
#pragma unroll
        for (int n = 0; n < 32; ++n) {
            const bf16x8 of = __builtin_bit_cast(bf16x8, O[n]);
            acc[0] = MFMA(WO[n & 1][0], of, acc[0]); if (n < 31) RD_O1((n + 1) & 1, 0, n + 1); SB();
            acc[1] = MFMA(WO[n & 1][1], of, acc[1]); if (n < 31) RD_O1((n + 1) & 1, 1, n + 1); SB();
        }
    }
    STAMP(10);
    const __amdgpu_buffer_rsrc_t rout = __builtin_amdgcn_make_buffer_rsrc(out, 0, 2048 * 1024, 0x00020000);
    {
        const float f = kh ? 1.f - g : g;
        const int tk = 32 * th + r;
#pragma unroll
        for (int nt = 0; nt < 2; ++nt)
#pragma unroll
            for (int q = 0; q < 4; ++q) {
                const int pc = 8 * nt + 2 * q + h;
                *(LAS f32x4*)(lds + L_F + kh * 16384 + tk * 256 + ((pc ^ (tk & 15)) << 4)) =
                    (f32x4){acc[nt][4 * q] * f, acc[nt][4 * q + 1] * f, acc[nt][4 * q + 2] * f, acc[nt][4 * q + 3] * f};
            }
        WAITL_BAR();
#pragma unroll
        for (int u = 0; u < 2; ++u) {
            const int tk2 = 16 * w + 8 * u + (lane >> 3), p8 = lane & 7;
            ldsp rowp = lds + L_F + tk2 * 256;
            const int c0 = ((2 * p8) ^ (tk2 & 15)) << 4, c1 = ((2 * p8 + 1) ^ (tk2 & 15)) << 4;
            const f32x4 a0 = *(const LAS f32x4*)(rowp + c0), a1 = *(const LAS f32x4*)(rowp + c1);
            const f32x4 b0 = *(const LAS f32x4*)(rowp + 16384 + c0), b1 = *(const LAS f32x4*)(rowp + 16384 + c1);
            const f32x4 s0 = a0 + b0, s1 = a1 + b1;
            const u32x4 o = {pk2(s0[0], s0[1]), pk2(s0[2], s0[3]), pk2(s1[0], s1[1]), pk2(s1[2], s1[3])};
            __builtin_amdgcn_raw_buffer_store_b128(o, rout, (unsigned)((t64 * 64 + tk2) * 1024 + n64 * 128 + p8 * 16), 0, 16);
        }
    }
    while (__builtin_amdgcn_s_memrealtime() - tpad0 < 3000) __builtin_amdgcn_s_sleep(2);
    STAMP(11);
    STAMP_OUT(12);
}

extern "C" void kernel_launch(void* const* d_in, const int* in_sizes, int n_in, void* d_out, int out_size, void* d_ws, size_t ws_size,
                              hipStream_t stream) {
    (void)in_sizes; (void)n_in; (void)out_size; (void)ws_size;
    (void)hipFuncSetAttribute((const void*)fused_kernel, hipFuncAttributeMaxDynamicSharedMemorySize, LDS_BYTES);
    fused_kernel<<<256, 512, LDS_BYTES, stream>>>((const u16*)d_in[0], (const u16*)d_in[1], (const u16*)d_in[2], (const u16*)d_in[4],
                                                  (const u16*)d_in[5], (const u16*)d_in[6], (u16*)d_out, (char*)d_ws);
}
```

```cpp
#include <hip/hip_runtime.h>
#include <hip/hip_bf16.h>
#include <stdint.h>

typedef unsigned short u16;
typedef unsigned long long u64;
typedef short bf16x8 __attribute__((ext_vector_type(8)));
typedef short s16x4 __attribute__((ext_vector_type(4)));
typedef float f32x16 __attribute__((ext_vector_type(16)));
typedef float f32x4 __attribute__((ext_vector_type(4)));
typedef float f32x2 __attribute__((ext_vector_type(2)));
typedef unsigned u32x4 __attribute__((ext_vector_type(4)));
typedef unsigned u32x2 __attribute__((ext_vector_type(2)));
typedef __bf16 bf16x2_t __attribute__((ext_vector_type(2)));
#define GAS __attribute__((address_space(1)))
#define LAS __attribute__((address_space(3)))
typedef GAS u64 gu64;
typedef GAS unsigned gu32;
typedef LAS char* ldsp;
#define DI __device__ __forceinline__
#define RLX_AGENT __ATOMIC_RELAXED, __HIP_MEMORY_SCOPE_AGENT
#define MFMA(a, b, c) __builtin_amdgcn_mfma_f32_32x32x16_bf16((a), (b), (c), 0, 0, 0)
#define SB() __builtin_amdgcn_sched_barrier(0)

#define N_DRAIN 8
#define OROT 0
#define N_G1 9
#define N_CHK2 6
#define N_POLL 27
#define N_ASYNC 6
#define ROTMODE 1

constexpr unsigned WS_E = 0;
constexpr unsigned WS_TMO = 256;
constexpr unsigned WS_F1 = 4096;
constexpr unsigned WS_F2 = WS_F1 + 32768;
constexpr unsigned WS_G = WS_F2 + 65536;
constexpr unsigned WS_P = WS_G + 16384;
constexpr unsigned PSZ = 2304;
constexpr unsigned WS_O = WS_P + 256 * PSZ;
constexpr unsigned WS_END = WS_O + 64 * 64 * 1024;
constexpr unsigned WS_STAMP = WS_END;

constexpr unsigned L_X = 0;
constexpr unsigned L_W = 49152;
constexpr unsigned L_WQ = 98304;
constexpr unsigned L_EX = 65536;
constexpr unsigned L_EXZ = 81920;
constexpr unsigned L_CTL = 86016;
constexpr unsigned L_KVZ = 90112;
constexpr unsigned L_F = 65536;
constexpr unsigned L_WOA = 0, L_WOB = 98304;
constexpr unsigned LDS_BYTES = 163840;
constexpr unsigned kSpinCap = 1u << 18;

#define STAMP_OUT(N) do { } while (0)
#define STAMP(n) do { } while (0)

DI unsigned pk2(float a, float b) { f32x2 v = {a, b}; return __builtin_bit_cast(unsigned, __builtin_convertvector(v, bf16x2_t)); }
constexpr float ONEP = 1.000001f, LOG2E = 1.4426950408889634f;
DI f32x2 phi2(f32x2 a) {
    const f32x2 u = __builtin_elementwise_fma(a, (f32x2){LOG2E, LOG2E}, (f32x2){-ONEP * LOG2E, -ONEP * LOG2E});
    const f32x2 e = {__builtin_amdgcn_fmed3f(__builtin_amdgcn_exp2f(u[0]), 0.f, 1.f), __builtin_amdgcn_fmed3f(__builtin_amdgcn_exp2f(u[1]), 0.f, 1.f)};
    const f32x2 c = e + 1e-6f;
    return (f32x2){__builtin_fmaxf(a[0], c[0]), __builtin_fmaxf(a[1], c[1])};
}
DI unsigned pkphi(float a, float b) { const f32x2 r = phi2((f32x2){a, b}); return pk2(r[0], r[1]); }
DI bf16x8 pack8(const f32x16& x, int s) {
    u32x4 p = {pk2(x[8 * s], x[8 * s + 1]), pk2(x[8 * s + 2], x[8 * s + 3]), pk2(x[8 * s + 4], x[8 * s + 5]), pk2(x[8 * s + 6], x[8 * s + 7])};
    return __builtin_bit_cast(bf16x8, p);
}
DI bf16x8 pack8phi(const f32x16& x, int s) {
    u32x4 p = {pkphi(x[8 * s], x[8 * s + 1]), pkphi(x[8 * s + 2], x[8 * s + 3]), pkphi(x[8 * s + 4], x[8 * s + 5]), pkphi(x[8 * s + 6], x[8 * s + 7])};
    return __builtin_bit_cast(bf16x8, p);
}
DI float dpp_add(float t, int ctrl_is_ror8) {
    const int i = __builtin_bit_cast(int, t);
    return t + __builtin_bit_cast(float, ctrl_is_ror8 ? __builtin_amdgcn_update_dpp(0, i, 0x128, 0xf, 0xf, true) : __builtin_amdgcn_update_dpp(0, i, 0x124, 0xf, 0xf, true));
}
DI float rows_sum(float t) {
    auto r16 = __builtin_amdgcn_permlane16_swap(__float_as_uint(t), __float_as_uint(t), false, false);
    t = __uint_as_float(r16[0]) + __uint_as_float(r16[1]);
    auto r32 = __builtin_amdgcn_permlane32_swap(__float_as_uint(t), __float_as_uint(t), false, false);
    return __uint_as_float(r32[0]) + __uint_as_float(r32[1]);
}
DI float wave_sum(float t) {
    int i = __builtin_bit_cast(int, t);
    t += __builtin_bit_cast(float, __builtin_amdgcn_update_dpp(0, i, 0x121, 0xf, 0xf, true)); i = __builtin_bit_cast(int, t);
    t += __builtin_bit_cast(float, __builtin_amdgcn_update_dpp(0, i, 0x122, 0xf, 0xf, true));
    t = dpp_add(t, 0); t = dpp_add(t, 1);
    return rows_sum(t);
}
DI bf16x8 lds_frag(ldsp p) { return *(const LAS bf16x8*)p; }
typedef short v4i16_t __attribute__((ext_vector_type(4)));
DI s16x4 vtr(ldsp p) { return __builtin_bit_cast(s16x4, __builtin_amdgcn_ds_read_tr16_b64_v4i16((LAS v4i16_t*)p)); }
DI bf16x8 tr_frag(ldsp p, int off0, int off1) {
    s16x4 lo = vtr(p + off0), hi = vtr(p + off1);
    return __builtin_shufflevector(lo, hi, 0, 1, 2, 3, 4, 5, 6, 7);
}
DI void glds(const void* sbase, unsigned voff, unsigned lds) {
    unsigned keep;
    asm volatile("s_mov_b32 %0, m0\n\ts_mov_b32 m0, %3\n\ts_nop 0\n\tglobal_load_lds_dwordx4 %1, %2\n\ts_mov_b32 m0, %0"
                 : "=&s"(keep) : "v"(voff), "s"(sbase), "s"(lds) : "memory");
}
DI void gld_sc1(u32x4& d, __amdgpu_buffer_rsrc_t r, unsigned voff, unsigned soff) {
    asm volatile("buffer_load_dwordx4 %0, %1, %2, %3 offen sc1" : "=v"(d) : "v"(voff), "s"(r), "s"(soff) : "memory");
}
#define WAITV_BAR(N) asm volatile("s_waitcnt vmcnt(" #N ")\n\ts_barrier" ::: "memory")
#define WAITL_BAR() asm volatile("s_waitcnt lgkmcnt(0)\n\ts_barrier" ::: "memory")
#define DRAIN() asm volatile("s_waitcnt vmcnt(0)" ::: "memory")

DI u64 read_epoch(const void* p) {
    u64 e;
    asm volatile("s_load_dwordx2 %0, %1, 0x0\n\ts_waitcnt lgkmcnt(0)" : "=s"(e) : "s"(p) : "memory");
    return e;
}
DI u64 flag_ld(GAS char* line, int lane) { return __hip_atomic_load((gu64*)(line + (lane & 15) * 8), RLX_AGENT); }
DI void poll16(GAS char* line, u64 tag, GAS char* ws, int lane) {
    u64 a = flag_ld(line, lane);
    for (unsigned spins = 0;; ++spins) {
        __builtin_amdgcn_s_sleep(3);
        const u64 c = flag_ld(line, lane);
        if (__all(a == tag)) break;
        if (spins > kSpinCap) { if (lane == 0) __hip_atomic_store((gu32*)(ws + WS_TMO), 1u, RLX_AGENT); break; }
        a = c;
    }
    asm volatile("" ::: "memory");
}

__global__ __launch_bounds__(512, 2) void fused_kernel(const u16* __restrict__ x, const u16* __restrict__ wA, const u16* __restrict__ woA,
                                                       const u16* __restrict__ wB, const u16* __restrict__ woB, const u16* __restrict__ gw,
                                                       u16* __restrict__ out, char* wsp) {
    extern __shared__ __attribute__((aligned(16))) char smem[];
    GAS char* ws = (GAS char*)wsp;
    const ldsp lds = (ldsp)smem;
    const unsigned lds0 = (unsigned)(uintptr_t)lds;
    const int wave = __builtin_amdgcn_readfirstlane(threadIdx.x >> 6), lane = threadIdx.x & 63;
    const int b = blockIdx.x, xcd = b & 7, idx = b >> 3, il = idx & 7, jl = idx >> 3;
    const int ti = (xcd & 1) * 8 + il, tj = (xcd >> 1) * 4 + jl;
    const int rot = ROTMODE == 0 ? 0 : ROTMODE == 1 ? ((il + 2 * jl) & 7) : ROTMODE == 2 ? 2 * ((il + jl) & 3) : 4 * ((il + jl) & 1);
    const int t64 = xcd * 4 + (idx & 3), n64 = idx >> 2;
    const __amdgpu_buffer_rsrc_t rws = __builtin_amdgcn_make_buffer_rsrc(wsp, 0, WS_END, 0x00020000);
    STAMP(0);

    if (wave >= 4) {
        const int lw = wave - 4;
        __builtin_amdgcn_s_setprio(3);
        const char* wbr = (const char*)((tj >> 3) ? wB : wA) + (tj & 7) * 128;
        const unsigned vx0 = (lane >> 3) * 1024 + (((lane & 7) ^ (lane >> 4)) << 4), vx1 = vx0 ^ 64;
        const unsigned vw = (lane >> 3) * 3072 + (((lane & 7) ^ (((lane >> 4) & 1) << 2)) << 4);
        const char* xs = (const char*)x + (size_t)(ti * 128 + lw * 32) * 1024;
        unsigned vo = (lane >> 3) * 1024 + (((lane & 7) ^ (((lane >> 4) & 1) << 2)) << 4);
        asm volatile("" : "+v"(vo));
        auto issue_kv = [&](int s) {
            const int cc = (s + rot) & 7, slot = (s + 2) % 3;
#pragma unroll
            for (int q = 0; q < 4; ++q) glds(xs + q * 8192 + cc * 128, (q & 1) ? vx1 : vx0, lds0 + L_X + slot * 16384 + lw * 4096 + q * 1024);
#pragma unroll
            for (int part = 0; part < 2; ++part)
#pragma unroll
                for (int q = 0; q < 2; ++q)
                    glds(wbr + (size_t)(cc * 64 + lw * 16 + q * 8) * 3072 + 1024 + part * 1024, vw, lds0 + L_W + slot * 16384 + part * 8192 + (lw * 2 + q) * 1024);
        };
        auto issue_q = [&](int s) {
            const int cc = (s + rot) & 7;
#pragma unroll
            for (int q = 0; q < 2; ++q) glds(wbr + (size_t)(cc * 64 + lw * 16 + q * 8) * 3072, vw, lds0 + L_WQ + s * 8192 + (lw * 2 + q) * 1024);
        };
        u64 epoch;
        asm volatile("s_load_dwordx2 %0, %1, 0x0" : "=s"(epoch) : "s"(wsp) : "memory");
        unsigned vg = (lane >> 5) * PSZ + (lane & 31) * 16, vz = (lane >> 2) * PSZ + (lane & 3) * 16, vf = (lane & 15) * 8;
        unsigned sl_ = 0x00003F80u, sh_ = 0x3F800000u;
        ldsp kvd = lds + L_KVZ + lw * 1024 + lane * 16, kzd = lds + L_KVZ + lw * 1024 + 512 + (lane & 1) * 16 + ((lane >> 1) & 1) * 8;
        asm volatile("" : "+v"(vg), "+v"(vz), "+v"(vf), "+v"(sl_), "+v"(sh_), "+v"(kvd), "+v"(kzd));
        issue_kv(0); issue_kv(1);
        WAITV_BAR(8);
        STAMP(1);
        issue_kv(2);
        WAITV_BAR(8);  issue_kv(3);
        WAITV_BAR(8);  issue_kv(4);
        WAITV_BAR(8);  issue_kv(5);
        WAITV_BAR(8);  issue_kv(6);
        WAITV_BAR(8);  issue_kv(7);
        WAITV_BAR(8);
        issue_q(0); issue_q(1); issue_q(2); issue_q(3);
        WAITV_BAR(8);
        STAMP(2);
        issue_q(4); issue_q(5); issue_q(6); issue_q(7);
        auto issue_wo = [&](const u16* wo, unsigned ldsb, int n0, int cnt) {
            const char* src = (const char*)wo + n64 * 128;
#pragma unroll
            for (int u = 0; u < cnt; ++u) { const int n = n0 + ((u + (cnt / 4) * (idx & 3)) & (cnt - 1)); glds(src + (size_t)n * 8192, vo, lds0 + ldsb + n * 1024); }
        };
        issue_wo(woA, L_WOA, 16 + lw * 8, 8);
        asm volatile("s_waitcnt lgkmcnt(0)" : "+s"(epoch) :: "memory");
        const u64 tag = epoch + 1;
        WAITV_BAR(8);
        STAMP(3);
        WAITV_BAR(63);
        issue_wo(woA, L_WOA, (lw < 2 ? 0 : 32) + lw * 8, 8);
        STAMP(5);
        WAITV_BAR(63);
        issue_wo(woB, L_WOB, lw * 8, 8);
        {
            GAS char* f1 = ws + WS_F1 + (tj * 16 + ti) * 128;
            for (unsigned spins = 0;; ++spins) {
                const u64 v = __hip_atomic_load((gu64*)(f1 + vf), RLX_AGENT);
                if (__all(v == tag)) break;
                if (spins > kSpinCap) { if (lane == 0) __hip_atomic_store((gu32*)(ws + WS_TMO), 1u, RLX_AGENT); break; }
                __builtin_amdgcn_s_sleep(1);
            }
            asm volatile("" ::: "memory");
            STAMP(6);
            const unsigned pb = WS_P + tj * 16 * PSZ;
            u32x4 gz, gv[8];
            gld_sc1(gz, rws, vz, pb + 2048 + lw * 64);
#pragma unroll
            for (int q = 0; q < 8; ++q) gld_sc1(gv[q], rws, vg, pb + 2 * q * PSZ + lw * 512);
            asm volatile("s_barrier" ::: "memory");
            issue_wo(woB, L_WOB, 32 + lw * 8, 8);
            asm volatile("s_waitcnt vmcnt(8)" : "+v"(gz), "+v"(gv[0]), "+v"(gv[1]), "+v"(gv[2]), "+v"(gv[3]), "+v"(gv[4]), "+v"(gv[5]), "+v"(gv[6]), "+v"(gv[7]) :: "memory");
            SB();
            f32x4 z = __builtin_bit_cast(f32x4, gz);
#pragma unroll
            for (int e = 0; e < 4; ++e) z[e] = rows_sum(dpp_add(dpp_add(z[e], 0), 1));
            if (lane < 4) *(LAS u32x2*)kzd = (u32x2){pk2(z[0], z[1]), pk2(z[2], z[3])};
            SB();
            const bf16x2_t sel_lo = __builtin_bit_cast(bf16x2_t, sl_), sel_hi = __builtin_bit_cast(bf16x2_t, sh_);
            float a[8];
#pragma unroll
            for (int e = 0; e < 8; ++e) a[e] = 0.f;
#pragma unroll
            for (int q = 0; q < 8; ++q)
#pragma unroll
                for (int e = 0; e < 4; ++e) {
                    const unsigned u = gv[q][e];
                    const bf16x2_t v = __builtin_bit_cast(bf16x2_t, u);
                    a[2 * e] = __builtin_amdgcn_fdot2_f32_bf16(v, sel_lo, a[2 * e], false);
                    a[2 * e + 1] = __builtin_amdgcn_fdot2_f32_bf16(v, sel_hi, a[2 * e + 1], false);
                }
#pragma unroll
            for (int e = 0; e < 8; ++e) {
                auto r32 = __builtin_amdgcn_permlane32_swap(__float_as_uint(a[e]), __float_as_uint(a[e]), false, false);
                a[e] = __uint_as_float(r32[0]) + __uint_as_float(r32[1]);
            }
            if (lane < 32) *(LAS u32x4*)kvd = (u32x4){pk2(a[0], a[1]), pk2(a[2], a[3]), pk2(a[4], a[5]), pk2(a[6], a[7])};
        }
        STAMP(4);
        WAITL_BAR();
        STAMP(7);
        WAITV_BAR(0);
        STAMP(8);
        STAMP_OUT(9);
        return;
    }

    const int w = wave, r = lane & 31, h = lane >> 5;
    const int q4 = (lane & 15) >> 2, p4 = lane & 3, ch = (lane >> 4) & 1, sw = (q4 >> 1) & 1;
    const u64 tag = read_epoch(wsp) + 1;
    ldsp xa[4];
    {
        const int row = 32 * w + r;
        const unsigned a0 = row * 128 + ((h ^ ((row >> 1) & 7)) << 4);
#pragma unroll
        for (int ks = 0; ks < 4; ++ks) xa[ks] = lds + L_X + (a0 ^ (ks << 5));
    }
    ldsp wb[2];
#pragma unroll
    for (int ct = 0; ct < 2; ++ct) wb[ct] = lds + L_W + (8 * h + q4) * 128 + ((ct ^ sw) * 64) + ch * 32 + p4 * 8;

    bf16x8 X[32];
    f32x16 aK[2], aV[2];
#pragma unroll
    for (int i = 0; i < 16; ++i) { aK[0][i] = ONEP; aK[1][i] = ONEP; aV[0][i] = 0.f; aV[1][i] = 0.f; }
    bf16x8 WK[2][2], WV[2][2];
#define OFFW(s, ks) ((((s) + 2) % 3) * 16384 + (ks) * 2048)
#define RD_K(buf, ct, s, ks) WK[buf][ct] = tr_frag(wb[ct], OFFW(s, ks), OFFW(s, ks) + 512)
#define RD_V(buf, ct, s, ks) WV[buf][ct] = tr_frag(wb[ct], OFFW(s, ks) + 8192, OFFW(s, ks) + 8192 + 512)
#define RD_X1(s, ks) X[4 * (s) + (ks)] = lds_frag(xa[ks] + (((s) + 2) % 3) * 16384)
    WAITL_BAR();
    STAMP(1);
    RD_X1(0, 0); RD_K(0, 0, 0, 0); RD_V(0, 0, 0, 0); RD_K(0, 1, 0, 0); RD_V(0, 1, 0, 0); RD_X1(0, 1); RD_X1(0, 2); RD_X1(0, 3);
    SB();
#pragma unroll
    for (int n = 0; n < 32; ++n) {
        const int ks = n & 3, cur = n & 1, nxt = cur ^ 1;
        const int s2 = (n + 1) >> 2, ks2 = (n + 1) & 3;
        if (ks == 3 && n < 31) { WAITL_BAR(); }
        aK[0] = MFMA(X[n], WK[cur][0], aK[0]); if (n < 31) { RD_K(nxt, 0, s2, ks2); RD_V(nxt, 0, s2, ks2); } SB();
        aV[0] = MFMA(X[n], WV[cur][0], aV[0]); if (n < 31) { RD_K(nxt, 1, s2, ks2); RD_V(nxt, 1, s2, ks2); } SB();
        aK[1] = MFMA(X[n], WK[cur][1], aK[1]); if (n < 31 && ks == 3) { RD_X1(s2, 0); RD_X1(s2, 1); } SB();
        aV[1] = MFMA(X[n], WV[cur][1], aV[1]); if (n < 31 && ks == 3) { RD_X1(s2, 2); RD_X1(s2, 3); } SB();
    }
    STAMP(2);
    {
        const bf16x8 ones = {0x3F80, 0x3F80, 0x3F80, 0x3F80, 0x3F80, 0x3F80, 0x3F80, 0x3F80};
        f32x16 zero;
#pragma unroll
        for (int i = 0; i < 16; ++i) zero[i] = 0.f;
        f32x16 ckv[2], cz[2];
        bf16x8 pk = pack8phi(aK[0], 0), vv = pack8(aV[0], 0);
        ckv[0] = MFMA(pk, vv, zero); cz[0] = MFMA(ones, pk, zero); SB();
        pk = pack8phi(aK[0], 1); vv = pack8(aV[0], 1);
        ckv[0] = MFMA(pk, vv, ckv[0]); cz[0] = MFMA(ones, pk, cz[0]); SB();
        pk = pack8phi(aK[1], 0); vv = pack8(aV[1], 0);
        ckv[1] = MFMA(pk, vv, zero); cz[1] = MFMA(ones, pk, zero); SB();
        pk = pack8phi(aK[1], 1); vv = pack8(aV[1], 1);
        const bool hi = (lane >> 4) & 1;
        ldsp dst = lds + L_EX + w * 4096 + (hi ? 1024 : 0) + (h * 16 + (lane & 15)) * 16;
        {
            const f32x16& c = ckv[0];
            *(LAS f32x4*)(dst) = hi ? (f32x4){c[8], c[9], c[10], c[11]} : (f32x4){c[0], c[1], c[2], c[3]};
            *(LAS f32x4*)(dst + 512) = hi ? (f32x4){c[12], c[13], c[14], c[15]} : (f32x4){c[4], c[5], c[6], c[7]};
            if (lane < 32) *(LAS float*)(lds + L_EXZ + w * 256 + lane * 4) = cz[0][0];
        }
        SB();
        ckv[1] = MFMA(pk, vv, ckv[1]); cz[1] = MFMA(ones, pk, cz[1]);
        if (w == 0 && lane < 3) *(LAS unsigned*)(lds + L_CTL + lane * 4) = 0u;
        {
            const f32x16& c = ckv[1];
            *(LAS f32x4*)(dst + 2048) = hi ? (f32x4){c[8], c[9], c[10], c[11]} : (f32x4){c[0], c[1], c[2], c[3]};
            *(LAS f32x4*)(dst + 2048 + 512) = hi ? (f32x4){c[12], c[13], c[14], c[15]} : (f32x4){c[4], c[5], c[6], c[7]};
            if (lane < 32) *(LAS float*)(lds + L_EXZ + w * 256 + 128 + lane * 4) = cz[1][0];
        }
    }
    WAITL_BAR();
    STAMP(3);
    {
        f32x4 a = *(const LAS f32x4*)(lds + L_EX + w * 1024 + lane * 16);
        float z = *(const LAS float*)(lds + L_EXZ + w * 64 + (lane & 15) * 4);
#pragma unroll
        for (int v = 1; v < 4; ++v) {
            a += *(const LAS f32x4*)(lds + L_EX + v * 4096 + w * 1024 + lane * 16);
            z += *(const LAS float*)(lds + L_EXZ + v * 256 + w * 64 + (lane & 15) * 4);
        }
        const unsigned pb = WS_P + (tj * 16 + ti) * PSZ;
        __builtin_amdgcn_raw_buffer_store_b64((u32x2){pk2(a[0], a[1]), pk2(a[2], a[3])}, rws, (lane & 31) * 16 + (lane >> 5) * 8, pb + w * 512, 16);
        if (lane < 16) __builtin_amdgcn_raw_buffer_store_b32(__builtin_bit_cast(unsigned, z), rws, lane * 4, pb + 2048 + w * 64, 16);
    }
    asm volatile("s_barrier" ::: "memory");
    SB();
    f32x16 aQ[2];
#pragma unroll
    for (int i = 0; i < 16; ++i) { aQ[0][i] = ONEP; aQ[1][i] = ONEP; }
    {
        ldsp wq0 = wb[0] + (L_WQ - L_W), wq1 = wb[1] + (L_WQ - L_W);
        asm volatile("" : "+v"(wq0), "+v"(wq1));
        bf16x8 WQ[3][2];
#define RD_Q1(buf, ct, n) WQ[buf][ct] = tr_frag((ct) ? wq1 : wq0, ((n) >> 2) * 8192 + ((n) & 3) * 2048, ((n) >> 2) * 8192 + ((n) & 3) * 2048 + 512)
        RD_Q1(0, 0, 0); RD_Q1(0, 1, 0); RD_Q1(1, 0, 1); RD_Q1(1, 1, 1); SB();
        unsigned old = 0;
#pragma unroll
        for (int n = 0; n < 32; ++n) {
            if (n == 16) { asm volatile("s_barrier" ::: "memory"); SB(); }
            if (n == N_DRAIN) {
                DRAIN();
                STAMP(15);
                if (lane == 0) old = __hip_atomic_fetch_add((LAS unsigned*)(lds + L_CTL), 1u, __ATOMIC_RELAXED, __HIP_MEMORY_SCOPE_WORKGROUP);
                SB();
            }
            if (n == N_DRAIN + 2) {
                if (__builtin_amdgcn_readfirstlane(old) == 3 && lane < 16) __hip_atomic_store((gu64*)(ws + WS_F1 + (tj * 16 + lane) * 128 + ti * 8), tag, RLX_AGENT);
                SB();
            }
            aQ[0] = MFMA(WQ[n % 3][0], X[n], aQ[0]); if (n < 30) RD_Q1((n + 2) % 3, 0, n + 2); SB();
            aQ[1] = MFMA(WQ[n % 3][1], X[n], aQ[1]); if (n < 30) RD_Q1((n + 2) % 3, 1, n + 2); SB();
        }
        STAMP(4);
    }
    WAITL_BAR();
    bf16x8 PQ[4];
#pragma unroll
    for (int hd = 0; hd < 4; ++hd) PQ[hd] = pack8phi(aQ[hd >> 1], hd & 1);
    asm volatile("" : "+v"(PQ[0]), "+v"(PQ[1]), "+v"(PQ[2]), "+v"(PQ[3]));
    SB();
    STAMP(5);
    WAITL_BAR();
    STAMP(6);
    {
        ldsp ka = lds + L_KVZ + (r < 16 ? (h * 16 + r) * 16 : 512 + h * 16);
        f32x16 zero;
#pragma unroll
        for (int i = 0; i < 16; ++i) zero[i] = 0.f;
        f32x16 c[4];
#pragma unroll
        for (int hd = 0; hd < 4; ++hd) c[hd] = MFMA(lds_frag(ka + hd * 1024), PQ[hd], zero);
        SB();
#pragma unroll
        for (int hd = 0; hd < 4; ++hd) {
            const float inv = __builtin_amdgcn_rcpf(c[hd][8] + 1e-6f);
            const u32x4 o = {pk2(c[hd][0] * inv, c[hd][1] * inv), pk2(c[hd][2] * inv, c[hd][3] * inv), pk2(c[hd][4] * inv, c[hd][5] * inv), pk2(c[hd][6] * inv, c[hd][7] * inv)};
            __builtin_amdgcn_raw_buffer_store_b128(o, rws, lane * 16, WS_O + ((ti * 4 + w) * 64 + tj * 4 + hd) * 1024, 16);
            SB();
        }
        STAMP(14);
        DRAIN();
        unsigned old = 0;
        if (lane == 0) old = __hip_atomic_fetch_add((LAS unsigned*)(lds + L_CTL + 4), 1u, __ATOMIC_RELAXED, __HIP_MEMORY_SCOPE_WORKGROUP);
        if (__builtin_amdgcn_readfirstlane(old) == 3 && lane < 16) __hip_atomic_store((gu64*)(ws + WS_F2 + (ti * 16 + lane) * 128 + tj * 8), tag, RLX_AGENT);
    }
    STAMP(7);
    const int th = w & 1, kh = w >> 1;
    const int gtok = ti * 128 + tj * 8 + w * 2;
    const u32x4 g4 = *(const u32x4*)((const char*)gw + lane * 16);
    const u32x4 gx0 = *(const u32x4*)((const char*)x + (size_t)gtok * 1024 + lane * 16);
    const u32x4 gx1 = *(const u32x4*)((const char*)x + (size_t)(gtok + 1) * 1024 + lane * 16);
    SB();
    WAITL_BAR();
    STAMP(8);
    GAS char* f2 = ws + WS_F2 + (t64 * 8 + n64) * 128;
    poll16(f2, tag, ws, lane);
    STAMP(9);
    if (b == 0 && w == 0 && lane == 0) __hip_atomic_store((gu64*)(ws + WS_E), tag, RLX_AGENT);
    constexpr int PF = 11;
    u32x4 O[32];
    const unsigned obase = WS_O + ((t64 * 2 + th) * 64 + kh * 32) * 1024;
    const int orot = OROT ? (((n64 * OROT) >> 3) * 32 / OROT) : 0;
#define LD_O(n) O[n] = __builtin_amdgcn_raw_buffer_load_b128(rws, lane * 16, obase + (((n) + orot) & 31) * 1024, 16)
#pragma unroll
    for (int n = 0; n < PF; ++n) LD_O(n);
    SB();
    {
        float s0 = 0.f, s1 = 0.f;
#pragma unroll
        for (int e = 0; e < 4; ++e) {
            const float gl = __uint_as_float(g4[e] << 16), gh = __uint_as_float(g4[e] & 0xffff0000u);
            s0 = __builtin_fmaf(__uint_as_float(gx0[e] << 16), gl, s0); s0 = __builtin_fmaf(__uint_as_float(gx0[e] & 0xffff0000u), gh, s0);
            s1 = __builtin_fmaf(__uint_as_float(gx1[e] << 16), gl, s1); s1 = __builtin_fmaf(__uint_as_float(gx1[e] & 0xffff0000u), gh, s1);
        }
        s0 = wave_sum(s0); s1 = wave_sum(s1);
        const float gv = __builtin_amdgcn_rcpf(1.f + __expf(-(lane ? s1 : s0)));
        if (lane < 2) __hip_atomic_store((gu64*)(ws + WS_G + (gtok + lane) * 8), (u64)__float_as_uint(gv) | (tag << 32), RLX_AGENT);
    }
    SB();
    STAMP(12);
    f32x16 acc[2];
#pragma unroll
    for (int i = 0; i < 16; ++i) { acc[0][i] = 0.f; acc[1][i] = 0.f; }
    u64 gg;
    gu64* gptr = (gu64*)(ws + WS_G + (t64 * 64 + th * 32 + r) * 8);
    {
        const unsigned ob0 = lds0 + (kh ? L_WOB : L_WOA) + (4 * h + q4) * 128 + ((0 ^ sw) * 64) + ch * 32 + p4 * 8;
        const unsigned ob1 = ob0 ^ 64;
        bf16x8 WO[3][2];
#define RD_O1(buf, nt, n) do { ldsp p_ = (ldsp)(uintptr_t)(((nt) ? ob1 : ob0) + (unsigned)((((n) + orot) & 31) * 2048)); WO[buf][nt] = tr_frag(p_, 0, 1024); } while (0)
        RD_O1(0, 0, 0); RD_O1(0, 1, 0); RD_O1(1, 0, 1); RD_O1(1, 1, 1); SB();
#pragma unroll
        for (int n = 0; n < 32; ++n) {
            const bf16x8 of = __builtin_bit_cast(bf16x8, O[n]);
            acc[0] = MFMA(WO[n % 3][0], of, acc[0]); if (n < 30) RD_O1((n + 2) % 3, 0, n + 2);
            if (n + PF < 32) LD_O(n + PF);
            if (n + PF == 32) gg = __hip_atomic_load(gptr, RLX_AGENT);
            SB();
            acc[1] = MFMA(WO[n % 3][1], of, acc[1]); if (n < 30) RD_O1((n + 2) % 3, 1, n + 2); SB();
        }
    }
    STAMP(10);
    {
        for (unsigned spins = 0; !__all((unsigned)(gg >> 32) == (unsigned)tag); ++spins) {
            if (spins > kSpinCap) { if (lane == 0) __hip_atomic_store((gu32*)(ws + WS_TMO), 1u, RLX_AGENT); break; }
            __builtin_amdgcn_s_sleep(1);
            gg = __hip_atomic_load(gptr, RLX_AGENT);
        }
        const float g = __uint_as_float((unsigned)gg);
        const float f = kh ? 1.f - g : g;
        const int tk = 32 * th + r;
#pragma unroll
        for (int nt = 0; nt < 2; ++nt)
#pragma unroll
            for (int q = 0; q < 4; ++q) {
                const int pc = 8 * nt + 2 * q + h;
                *(LAS f32x4*)(lds + L_F + kh * 16384 + tk * 256 + ((pc ^ (tk & 15)) << 4)) =
                    (f32x4){acc[nt][4 * q] * f, acc[nt][4 * q + 1] * f, acc[nt][4 * q + 2] * f, acc[nt][4 * q + 3] * f};
            }
        WAITL_BAR();
#pragma unroll
        for (int u = 0; u < 2; ++u) {
            const int tk2 = 16 * w + 8 * u + (lane >> 3), p8 = lane & 7;
            ldsp rowp = lds + L_F + tk2 * 256;
            const int c0 = ((2 * p8) ^ (tk2 & 15)) << 4, c1 = ((2 * p8 + 1) ^ (tk2 & 15)) << 4;
            const f32x4 a0 = *(const LAS f32x4*)(rowp + c0), a1 = *(const LAS f32x4*)(rowp + c1);
            const f32x4 b0 = *(const LAS f32x4*)(rowp + 16384 + c0), b1 = *(const LAS f32x4*)(rowp + 16384 + c1);
            const f32x4 s0 = a0 + b0, s1 = a1 + b1;
            const u32x4 o = {pk2(s0[0], s0[1]), pk2(s0[2], s0[3]), pk2(s1[0], s1[1]), pk2(s1[2], s1[3])};
            *(u32x4*)((char*)out + (size_t)(t64 * 64 + tk2) * 1024 + n64 * 128 + p8 * 16) = o;
        }
    }
    STAMP(11);
    STAMP_OUT(18);
}

extern "C" void kernel_launch(void* const* d_in, const int* in_sizes, int n_in, void* d_out, int out_size, void* d_ws, size_t ws_size,
                              hipStream_t stream) {
    (void)in_sizes; (void)n_in; (void)out_size; (void)ws_size;
    (void)hipFuncSetAttribute((const void*)fused_kernel, hipFuncAttributeMaxDynamicSharedMemorySize, LDS_BYTES);
    fused_kernel<<<256, 512, LDS_BYTES, stream>>>((const u16*)d_in[0], (const u16*)d_in[1], (const u16*)d_in[2], (const u16*)d_in[4],
                                                  (const u16*)d_in[5], (const u16*)d_in[6], (u16*)d_out, (char*)d_ws);
}
```

```cpp
#include <hip/hip_runtime.h>
#include <hip/hip_bf16.h>
#include <stdint.h>

typedef unsigned short u16;
typedef unsigned long long u64;
typedef short bf16x8 __attribute__((ext_vector_type(8)));
typedef short s16x4 __attribute__((ext_vector_type(4)));
typedef float f32x16 __attribute__((ext_vector_type(16)));
typedef float f32x4 __attribute__((ext_vector_type(4)));
typedef float f32x2 __attribute__((ext_vector_type(2)));
typedef unsigned u32x4 __attribute__((ext_vector_type(4)));
typedef unsigned u32x2 __attribute__((ext_vector_type(2)));
typedef __bf16 bf16x2_t __attribute__((ext_vector_type(2)));
#define GAS __attribute__((address_space(1)))
#define LAS __attribute__((address_space(3)))
typedef GAS u64 gu64;
typedef GAS unsigned gu32;
typedef LAS char* ldsp;
#define DI __device__ __forceinline__
#define RLX_AGENT __ATOMIC_RELAXED, __HIP_MEMORY_SCOPE_AGENT
#define MFMA(a, b, c) __builtin_amdgcn_mfma_f32_32x32x16_bf16((a), (b), (c), 0, 0, 0)
#define SB() __builtin_amdgcn_sched_barrier(0)

#define N_DRAIN 8
#define OROT 0
#define N_G1 9
#define N_CHK2 6
#define N_POLL 27
#define N_ASYNC 6
#define ROTMODE 3

constexpr unsigned WS_E = 0;
constexpr unsigned WS_TMO = 256;
constexpr unsigned WS_F1 = 4096;
constexpr unsigned WS_F2 = WS_F1 + 32768;
constexpr unsigned WS_G = WS_F2 + 65536;
constexpr unsigned WS_P = WS_G + 16384;
constexpr unsigned PSZ = 2304;
constexpr unsigned WS_O = WS_P + 256 * PSZ;
constexpr unsigned WS_END = WS_O + 64 * 64 * 1024;
constexpr unsigned WS_STAMP = WS_END;

constexpr unsigned L_X = 0;
constexpr unsigned L_W = 49152;
constexpr unsigned L_WQ = 98304;
constexpr unsigned L_EX = 65536;
constexpr unsigned L_EXZ = 81920;
constexpr unsigned L_CTL = 86016;
constexpr unsigned L_KVZ = 90112;
constexpr unsigned L_F = 65536;
constexpr unsigned L_WOA = 0, L_WOB = 98304;
constexpr unsigned LDS_BYTES = 163840;
constexpr unsigned kSpinCap = 1u << 18;

#define STAMP_OUT(N) do { } while (0)
#define STAMP(n) do { } while (0)

DI unsigned pk2(float a, float b) { f32x2 v = {a, b}; return __builtin_bit_cast(unsigned, __builtin_convertvector(v, bf16x2_t)); }
constexpr float ONEP = 1.000001f, LOG2E = 1.4426950408889634f;
DI f32x2 phi2(f32x2 a) {
    const f32x2 u = __builtin_elementwise_fma(a, (f32x2){LOG2E, LOG2E}, (f32x2){-ONEP * LOG2E, -ONEP * LOG2E});
    const f32x2 e = {__builtin_amdgcn_fmed3f(__builtin_amdgcn_exp2f(u[0]), 0.f, 1.f), __builtin_amdgcn_fmed3f(__builtin_amdgcn_exp2f(u[1]), 0.f, 1.f)};
    const f32x2 c = e + 1e-6f;
    return (f32x2){__builtin_fmaxf(a[0], c[0]), __builtin_fmaxf(a[1], c[1])};
}
DI unsigned pkphi(float a, float b) { const f32x2 r = phi2((f32x2){a, b}); return pk2(r[0], r[1]); }
DI bf16x8 pack8(const f32x16& x, int s) {
    u32x4 p = {pk2(x[8 * s], x[8 * s + 1]), pk2(x[8 * s + 2], x[8 * s + 3]), pk2(x[8 * s + 4], x[8 * s + 5]), pk2(x[8 * s + 6], x[8 * s + 7])};
    return __builtin_bit_cast(bf16x8, p);
}
DI bf16x8 pack8phi(const f32x16& x, int s) {
    u32x4 p = {pkphi(x[8 * s], x[8 * s + 1]), pkphi(x[8 * s + 2], x[8 * s + 3]), pkphi(x[8 * s + 4], x[8 * s + 5]), pkphi(x[8 * s + 6], x[8 * s + 7])};
    return __builtin_bit_cast(bf16x8, p);
}
DI float dpp_add(float t, int ctrl_is_ror8) {
    const int i = __builtin_bit_cast(int, t);
    return t + __builtin_bit_cast(float, ctrl_is_ror8 ? __builtin_amdgcn_update_dpp(0, i, 0x128, 0xf, 0xf, true) : __builtin_amdgcn_update_dpp(0, i, 0x124, 0xf, 0xf, true));
}
DI float rows_sum(float t) {
    auto r16 = __builtin_amdgcn_permlane16_swap(__float_as_uint(t), __float_as_uint(t), false, false);
    t = __uint_as_float(r16[0]) + __uint_as_float(r16[1]);
    auto r32 = __builtin_amdgcn_permlane32_swap(__float_as_uint(t), __float_as_uint(t), false, false);
    return __uint_as_float(r32[0]) + __uint_as_float(r32[1]);
}
DI float wave_sum(float t) {
    int i = __builtin_bit_cast(int, t);
    t += __builtin_bit_cast(float, __builtin_amdgcn_update_dpp(0, i, 0x121, 0xf, 0xf, true)); i = __builtin_bit_cast(int, t);
    t += __builtin_bit_cast(float, __builtin_amdgcn_update_dpp(0, i, 0x122, 0xf, 0xf, true));
    t = dpp_add(t, 0); t = dpp_add(t, 1);
    return rows_sum(t);
}
DI bf16x8 lds_frag(ldsp p) { return *(const LAS bf16x8*)p; }
typedef short v4i16_t __attribute__((ext_vector_type(4)));
DI s16x4 vtr(ldsp p) { return __builtin_bit_cast(s16x4, __builtin_amdgcn_ds_read_tr16_b64_v4i16((LAS v4i16_t*)p)); }
DI bf16x8 tr_frag(ldsp p, int off0, int off1) {
    s16x4 lo = vtr(p + off0), hi = vtr(p + off1);
    return __builtin_shufflevector(lo, hi, 0, 1, 2, 3, 4, 5, 6, 7);
}
DI void glds(const void* sbase, unsigned voff, unsigned lds) {
    unsigned keep;
    asm volatile("s_mov_b32 %0, m0\n\ts_mov_b32 m0, %3\n\ts_nop 0\n\tglobal_load_lds_dwordx4 %1, %2\n\ts_mov_b32 m0, %0"
                 : "=&s"(keep) : "v"(voff), "s"(sbase), "s"(lds) : "memory");
}
DI void gld_sc1(u32x4& d, __amdgpu_buffer_rsrc_t r, unsigned voff, unsigned soff) {
    asm volatile("buffer_load_dwordx4 %0, %1, %2, %3 offen sc1" : "=v"(d) : "v"(voff), "s"(r), "s"(soff) : "memory");
}
#define WAITV_BAR(N) asm volatile("s_waitcnt vmcnt(" #N ")\n\ts_barrier" ::: "memory")
#define WAITL_BAR() asm volatile("s_waitcnt lgkmcnt(0)\n\ts_barrier" ::: "memory")
#define DRAIN() asm volatile("s_waitcnt vmcnt(0)" ::: "memory")

DI u64 read_epoch(const void* p) {
    u64 e;
    asm volatile("s_load_dwordx2 %0, %1, 0x0\n\ts_waitcnt lgkmcnt(0)" : "=s"(e) : "s"(p) : "memory");
    return e;
}
DI u64 flag_ld(GAS char* line, int lane) { return __hip_atomic_load((gu64*)(line + (lane & 15) * 8), RLX_AGENT); }
DI void poll16(GAS char* line, u64 tag, GAS char* ws, int lane) {
    u64 a = flag_ld(line, lane);
    for (unsigned spins = 0;; ++spins) {
        __builtin_amdgcn_s_sleep(3);
        const u64 c = flag_ld(line, lane);
        if (__all(a == tag)) break;
        if (spins > kSpinCap) { if (lane == 0) __hip_atomic_store((gu32*)(ws + WS_TMO), 1u, RLX_AGENT); break; }
        a = c;
    }
    asm volatile("" ::: "memory");
}

__global__ __launch_bounds__(512, 2) void fused_kernel(const u16* __restrict__ x, const u16* __restrict__ wA, const u16* __restrict__ woA,
                                                       const u16* __restrict__ wB, const u16* __restrict__ woB, const u16* __restrict__ gw,
                                                       u16* __restrict__ out, char* wsp) {
    extern __shared__ __attribute__((aligned(16))) char smem[];
    GAS char* ws = (GAS char*)wsp;
    const ldsp lds = (ldsp)smem;
    const unsigned lds0 = (unsigned)(uintptr_t)lds;
    const int wave = __builtin_amdgcn_readfirstlane(threadIdx.x >> 6), lane = threadIdx.x & 63;
    const int b = blockIdx.x, xcd = b & 7, idx = b >> 3, il = idx & 7, jl = idx >> 3;
    const int ti = (xcd & 1) * 8 + il, tj = (xcd >> 1) * 4 + jl;
    const int rot = ROTMODE == 0 ? 0 : ROTMODE == 1 ? ((il + 2 * jl) & 7) : ROTMODE == 2 ? 2 * ((il + jl) & 3) : 4 * ((il + jl) & 1);
    const int t64 = xcd * 4 + (idx & 3), n64 = idx >> 2;
    const __amdgpu_buffer_rsrc_t rws = __builtin_amdgcn_make_buffer_rsrc(wsp, 0, WS_END, 0x00020000);
    STAMP(0);

    if (wave >= 4) {
        const int lw = wave - 4;
        __builtin_amdgcn_s_setprio(3);
        const char* wbr = (const char*)((tj >> 3) ? wB : wA) + (tj & 7) * 128;
        const unsigned vx0 = (lane >> 3) * 1024 + (((lane & 7) ^ (lane >> 4)) << 4), vx1 = vx0 ^ 64;
        const unsigned vw = (lane >> 3) * 3072 + (((lane & 7) ^ (((lane >> 4) & 1) << 2)) << 4);
        const char* xs = (const char*)x + (size_t)(ti * 128 + lw * 32) * 1024;
        unsigned vo = (lane >> 3) * 1024 + (((lane & 7) ^ (((lane >> 4) & 1) << 2)) << 4);
        asm volatile("" : "+v"(vo));
        auto issue_kv = [&](int s) {
            const int cc = (s + rot) & 7, slot = (s + 2) % 3;
#pragma unroll
            for (int q = 0; q < 4; ++q) glds(xs + q * 8192 + cc * 128, (q & 1) ? vx1 : vx0, lds0 + L_X + slot * 16384 + lw * 4096 + q * 1024);
#pragma unroll
            for (int part = 0; part < 2; ++part)
#pragma unroll
                for (int q = 0; q < 2; ++q)
                    glds(wbr + (size_t)(cc * 64 + lw * 16 + q * 8) * 3072 + 1024 + part * 1024, vw, lds0 + L_W + slot * 16384 + part * 8192 + (lw * 2 + q) * 1024);
        };
        auto issue_q = [&](int s) {
            const int cc = (s + rot) & 7;
#pragma unroll
            for (int q = 0; q < 2; ++q) glds(wbr + (size_t)(cc * 64 + lw * 16 + q * 8) * 3072, vw, lds0 + L_WQ + s * 8192 + (lw * 2 + q) * 1024);
        };
        asm volatile("" :: "s"(woA), "s"(woB), "s"(wsp), "s"(out), "s"(gw));
        unsigned vg = (lane >> 5) * PSZ + (lane & 31) * 16, vz = (lane >> 2) * PSZ + (lane & 3) * 16, vf = (lane & 15) * 8;
        unsigned sl_ = 0x00003F80u, sh_ = 0x3F800000u;
        ldsp kvd = lds + L_KVZ + lw * 1024 + lane * 16, kzd = lds + L_KVZ + lw * 1024 + 512 + (lane & 1) * 16 + ((lane >> 1) & 1) * 8;
        asm volatile("" : "+v"(vg), "+v"(vz), "+v"(vf), "+v"(sl_), "+v"(sh_), "+v"(kvd), "+v"(kzd));
        issue_kv(0); issue_kv(1);
        u64 epoch;
        asm volatile("s_load_dwordx2 %0, %1, 0x0" : "=s"(epoch) : "s"(wsp) : "memory");
        WAITV_BAR(8);
        STAMP(1);
        issue_kv(2);
        WAITV_BAR(8);  issue_kv(3);
        WAITV_BAR(8);  issue_kv(4);
        WAITV_BAR(8);  issue_kv(5);
        WAITV_BAR(8);  issue_kv(6);
        WAITV_BAR(8);  issue_kv(7);
        WAITV_BAR(8);
        issue_q(0); issue_q(1); issue_q(2); issue_q(3);
        WAITV_BAR(8);
        STAMP(2);
        issue_q(4); issue_q(5); issue_q(6); issue_q(7);
        auto issue_wo = [&](const u16* wo, unsigned ldsb, int n0, int cnt) {
            const char* src = (const char*)wo + n64 * 128;
#pragma unroll
            for (int u = 0; u < cnt; ++u) { const int n = n0 + ((u + (cnt / 4) * (idx & 3)) & (cnt - 1)); glds(src + (size_t)n * 8192, vo, lds0 + ldsb + n * 1024); }
        };
        issue_wo(woA, L_WOA, 16 + lw * 8, 8);
        asm volatile("s_waitcnt lgkmcnt(0)" : "+s"(epoch) :: "memory");
        const u64 tag = epoch + 1;
        if (lw == 0 && lane == 0) *(LAS u64*)(lds + L_CTL + 16) = tag;
        asm volatile("s_waitcnt lgkmcnt(0)" ::: "memory");
        WAITV_BAR(8);
        STAMP(3);
        WAITV_BAR(63);
        issue_wo(woA, L_WOA, (lw < 2 ? 0 : 32) + lw * 8, 8);
        STAMP(5);
        WAITV_BAR(63);
        issue_wo(woB, L_WOB, lw * 8, 8);
        {
            GAS char* f1 = ws + WS_F1 + (tj * 16 + ti) * 128;
            for (unsigned spins = 0;; ++spins) {
                const u64 v = __hip_atomic_load((gu64*)(f1 + vf), RLX_AGENT);
                if (__all(v == tag)) break;
                if (spins > kSpinCap) { if (lane == 0) __hip_atomic_store((gu32*)(ws + WS_TMO), 1u, RLX_AGENT); break; }
                __builtin_amdgcn_s_sleep(1);
            }
            asm volatile("" ::: "memory");
            STAMP(6);
            const unsigned pb = WS_P + tj * 16 * PSZ;
            u32x4 gz, gv[8];
            gld_sc1(gz, rws, vz, pb + 2048 + lw * 64);
#pragma unroll
            for (int q = 0; q < 8; ++q) gld_sc1(gv[q], rws, vg, pb + 2 * q * PSZ + lw * 512);
            asm volatile("s_barrier" ::: "memory");
            issue_wo(woB, L_WOB, 32 + lw * 8, 8);
            asm volatile("s_waitcnt vmcnt(8)" : "+v"(gz), "+v"(gv[0]), "+v"(gv[1]), "+v"(gv[2]), "+v"(gv[3]), "+v"(gv[4]), "+v"(gv[5]), "+v"(gv[6]), "+v"(gv[7]) :: "memory");
            SB();
            f32x4 z = __builtin_bit_cast(f32x4, gz);
#pragma unroll
            for (int e = 0; e < 4; ++e) z[e] = rows_sum(dpp_add(dpp_add(z[e], 0), 1));
            if (lane < 4) *(LAS u32x2*)kzd = (u32x2){pk2(z[0], z[1]), pk2(z[2], z[3])};
            SB();
            const bf16x2_t sel_lo = __builtin_bit_cast(bf16x2_t, sl_), sel_hi = __builtin_bit_cast(bf16x2_t, sh_);
            float a[8];
#pragma unroll
            for (int e = 0; e < 8; ++e) a[e] = 0.f;
#pragma unroll
            for (int q = 0; q < 8; ++q)
#pragma unroll
                for (int e = 0; e < 4; ++e) {
                    const unsigned u = gv[q][e];
                    const bf16x2_t v = __builtin_bit_cast(bf16x2_t, u);
                    a[2 * e] = __builtin_amdgcn_fdot2_f32_bf16(v, sel_lo, a[2 * e], false);
                    a[2 * e + 1] = __builtin_amdgcn_fdot2_f32_bf16(v, sel_hi, a[2 * e + 1], false);
                }
#pragma unroll
            for (int e = 0; e < 8; ++e) {
                auto r32 = __builtin_amdgcn_permlane32_swap(__float_as_uint(a[e]), __float_as_uint(a[e]), false, false);
                a[e] = __uint_as_float(r32[0]) + __uint_as_float(r32[1]);
            }
            if (lane < 32) *(LAS u32x4*)kvd = (u32x4){pk2(a[0], a[1]), pk2(a[2], a[3]), pk2(a[4], a[5]), pk2(a[6], a[7])};
        }
        STAMP(4);
        WAITL_BAR();
        STAMP(7);
        WAITV_BAR(0);
        STAMP(8);
        STAMP_OUT(9);
        return;
    }

    const int w = wave, r = lane & 31, h = lane >> 5;
    const int q4 = (lane & 15) >> 2, p4 = lane & 3, ch = (lane >> 4) & 1, sw = (q4 >> 1) & 1;
    ldsp xa[4];
    {
        const int row = 32 * w + r;
        const unsigned a0 = row * 128 + ((h ^ ((row >> 1) & 7)) << 4);
#pragma unroll
        for (int ks = 0; ks < 4; ++ks) xa[ks] = lds + L_X + (a0 ^ (ks << 5));
    }
    ldsp wb[2];
#pragma unroll
    for (int ct = 0; ct < 2; ++ct) wb[ct] = lds + L_W + (8 * h + q4) * 128 + ((ct ^ sw) * 64) + ch * 32 + p4 * 8;

    bf16x8 X[32];
    f32x16 aK[2], aV[2];
#pragma unroll
    for (int i = 0; i < 16; ++i) { aK[0][i] = ONEP; aK[1][i] = ONEP; aV[0][i] = 0.f; aV[1][i] = 0.f; }
    bf16x8 WK[2][2], WV[2][2];
#define OFFW(s, ks) ((((s) + 2) % 3) * 16384 + (ks) * 2048)
#define RD_K(buf, ct, s, ks) WK[buf][ct] = tr_frag(wb[ct], OFFW(s, ks), OFFW(s, ks) + 512)
#define RD_V(buf, ct, s, ks) WV[buf][ct] = tr_frag(wb[ct], OFFW(s, ks) + 8192, OFFW(s, ks) + 8192 + 512)
#define RD_X1(s, ks) X[4 * (s) + (ks)] = lds_frag(xa[ks] + (((s) + 2) % 3) * 16384)
    WAITL_BAR();
    STAMP(1);
    RD_X1(0, 0); RD_K(0, 0, 0, 0); RD_V(0, 0, 0, 0); RD_K(0, 1, 0, 0); RD_V(0, 1, 0, 0); RD_X1(0, 1); RD_X1(0, 2); RD_X1(0, 3);
    SB();
#pragma unroll
    for (int n = 0; n < 32; ++n) {
        const int ks = n & 3, cur = n & 1, nxt = cur ^ 1;
        const int s2 = (n + 1) >> 2, ks2 = (n + 1) & 3;
        if (ks == 3 && n < 31) { WAITL_BAR(); }
        aK[0] = MFMA(X[n], WK[cur][0], aK[0]); if (n < 31) { RD_K(nxt, 0, s2, ks2); RD_V(nxt, 0, s2, ks2); } SB();
        aV[0] = MFMA(X[n], WV[cur][0], aV[0]); if (n < 31) { RD_K(nxt, 1, s2, ks2); RD_V(nxt, 1, s2, ks2); } SB();
        aK[1] = MFMA(X[n], WK[cur][1], aK[1]); if (n < 31 && ks == 3) { RD_X1(s2, 0); RD_X1(s2, 1); } SB();
        aV[1] = MFMA(X[n], WV[cur][1], aV[1]); if (n < 31 && ks == 3) { RD_X1(s2, 2); RD_X1(s2, 3); } SB();
    }
    STAMP(2);
    {
        const bf16x8 ones = {0x3F80, 0x3F80, 0x3F80, 0x3F80, 0x3F80, 0x3F80, 0x3F80, 0x3F80};
        f32x16 zero;
#pragma unroll
        for (int i = 0; i < 16; ++i) zero[i] = 0.f;
        f32x16 ckv[2], cz[2];
        bf16x8 pk = pack8phi(aK[0], 0), vv = pack8(aV[0], 0);
        ckv[0] = MFMA(pk, vv, zero); cz[0] = MFMA(ones, pk, zero); SB();
        pk = pack8phi(aK[0], 1); vv = pack8(aV[0], 1);
        ckv[0] = MFMA(pk, vv, ckv[0]); cz[0] = MFMA(ones, pk, cz[0]); SB();
        pk = pack8phi(aK[1], 0); vv = pack8(aV[1], 0);
        ckv[1] = MFMA(pk, vv, zero); cz[1] = MFMA(ones, pk, zero); SB();
        pk = pack8phi(aK[1], 1); vv = pack8(aV[1], 1);
        const bool hi = (lane >> 4) & 1;
        ldsp dst = lds + L_EX + w * 4096 + (hi ? 1024 : 0) + (h * 16 + (lane & 15)) * 16;
        {
            const f32x16& c = ckv[0];
            *(LAS f32x4*)(dst) = hi ? (f32x4){c[8], c[9], c[10], c[11]} : (f32x4){c[0], c[1], c[2], c[3]};
            *(LAS f32x4*)(dst + 512) = hi ? (f32x4){c[12], c[13], c[14], c[15]} : (f32x4){c[4], c[5], c[6], c[7]};
            if (lane < 32) *(LAS float*)(lds + L_EXZ + w * 256 + lane * 4) = cz[0][0];
        }
        SB();
        ckv[1] = MFMA(pk, vv, ckv[1]); cz[1] = MFMA(ones, pk, cz[1]);
        if (w == 0 && lane < 3) *(LAS unsigned*)(lds + L_CTL + lane * 4) = 0u;
        {
            const f32x16& c = ckv[1];
            *(LAS f32x4*)(dst + 2048) = hi ? (f32x4){c[8], c[9], c[10], c[11]} : (f32x4){c[0], c[1], c[2], c[3]};
            *(LAS f32x4*)(dst + 2048 + 512) = hi ? (f32x4){c[12], c[13], c[14], c[15]} : (f32x4){c[4], c[5], c[6], c[7]};
            if (lane < 32) *(LAS float*)(lds + L_EXZ + w * 256 + 128 + lane * 4) = cz[1][0];
        }
    }
    WAITL_BAR();
    STAMP(3);
    u64 tag;
    {
        const u32x2 tv = *(const LAS u32x2*)(lds + L_CTL + 16);
        tag = (u64)(unsigned)__builtin_amdgcn_readfirstlane(tv[0]) | ((u64)(unsigned)__builtin_amdgcn_readfirstlane(tv[1]) << 32);
    }
    {
        f32x4 a = *(const LAS f32x4*)(lds + L_EX + w * 1024 + lane * 16);
        float z = *(const LAS float*)(lds + L_EXZ + w * 64 + (lane & 15) * 4);
#pragma unroll
        for (int v = 1; v < 4; ++v) {
            a += *(const LAS f32x4*)(lds + L_EX + v * 4096 + w * 1024 + lane * 16);
            z += *(const LAS float*)(lds + L_EXZ + v * 256 + w * 64 + (lane & 15) * 4);
        }
        const unsigned pb = WS_P + (tj * 16 + ti) * PSZ;
        __builtin_amdgcn_raw_buffer_store_b64((u32x2){pk2(a[0], a[1]), pk2(a[2], a[3])}, rws, (lane & 31) * 16 + (lane >> 5) * 8, pb + w * 512, 16);
        if (lane < 16) __builtin_amdgcn_raw_buffer_store_b32(__builtin_bit_cast(unsigned, z), rws, lane * 4, pb + 2048 + w * 64, 16);
    }
    asm volatile("s_barrier" ::: "memory");
    SB();
    f32x16 aQ[2];
#pragma unroll
    for (int i = 0; i < 16; ++i) { aQ[0][i] = ONEP; aQ[1][i] = ONEP; }
    {
        ldsp wq0 = wb[0] + (L_WQ - L_W), wq1 = wb[1] + (L_WQ - L_W);
        asm volatile("" : "+v"(wq0), "+v"(wq1));
        bf16x8 WQ[3][2];
#define RD_Q1(buf, ct, n) WQ[buf][ct] = tr_frag((ct) ? wq1 : wq0, ((n) >> 2) * 8192 + ((n) & 3) * 2048, ((n) >> 2) * 8192 + ((n) & 3) * 2048 + 512)
        RD_Q1(0, 0, 0); RD_Q1(0, 1, 0); RD_Q1(1, 0, 1); RD_Q1(1, 1, 1); SB();
        unsigned old = 0;
#pragma unroll
        for (int n = 0; n < 32; ++n) {
            if (n == 16) { asm volatile("s_barrier" ::: "memory"); SB(); }
            if (n == N_DRAIN) {
                DRAIN();
                STAMP(15);
                if (lane == 0) old = __hip_atomic_fetch_add((LAS unsigned*)(lds + L_CTL), 1u, __ATOMIC_RELAXED, __HIP_MEMORY_SCOPE_WORKGROUP);
                SB();
            }
            if (n == N_DRAIN + 2) {
                if (__builtin_amdgcn_readfirstlane(old) == 3 && lane < 16) __hip_atomic_store((gu64*)(ws + WS_F1 + (tj * 16 + lane) * 128 + ti * 8), tag, RLX_AGENT);
                SB();
            }
            aQ[0] = MFMA(WQ[n % 3][0], X[n], aQ[0]); if (n < 30) RD_Q1((n + 2) % 3, 0, n + 2); SB();
            aQ[1] = MFMA(WQ[n % 3][1], X[n], aQ[1]); if (n < 30) RD_Q1((n + 2) % 3, 1, n + 2); SB();
        }
        STAMP(4);
    }
    WAITL_BAR();
    bf16x8 PQ[4];
#pragma unroll
    for (int hd = 0; hd < 4; ++hd) PQ[hd] = pack8phi(aQ[hd >> 1], hd & 1);
    asm volatile("" : "+v"(PQ[0]), "+v"(PQ[1]), "+v"(PQ[2]), "+v"(PQ[3]));
    SB();
    STAMP(5);
    WAITL_BAR();
    STAMP(6);
    {
        ldsp ka = lds + L_KVZ + (r < 16 ? (h * 16 + r) * 16 : 512 + h * 16);
        f32x16 zero;
#pragma unroll
        for (int i = 0; i < 16; ++i) zero[i] = 0.f;
        f32x16 c[4];
#pragma unroll
        for (int hd = 0; hd < 4; ++hd) c[hd] = MFMA(lds_frag(ka + hd * 1024), PQ[hd], zero);
        SB();
#pragma unroll
        for (int hd = 0; hd < 4; ++hd) {
            const float inv = __builtin_amdgcn_rcpf(c[hd][8] + 1e-6f);
            const u32x4 o = {pk2(c[hd][0] * inv, c[hd][1] * inv), pk2(c[hd][2] * inv, c[hd][3] * inv), pk2(c[hd][4] * inv, c[hd][5] * inv), pk2(c[hd][6] * inv, c[hd][7] * inv)};
            __builtin_amdgcn_raw_buffer_store_b128(o, rws, lane * 16, WS_O + ((ti * 4 + w) * 64 + tj * 4 + hd) * 1024, 16);
            SB();
        }
        STAMP(14);
        DRAIN();
        unsigned old = 0;
        if (lane == 0) old = __hip_atomic_fetch_add((LAS unsigned*)(lds + L_CTL + 4), 1u, __ATOMIC_RELAXED, __HIP_MEMORY_SCOPE_WORKGROUP);
        if (__builtin_amdgcn_readfirstlane(old) == 3 && lane < 16) __hip_atomic_store((gu64*)(ws + WS_F2 + (ti * 16 + lane) * 128 + tj * 8), tag, RLX_AGENT);
    }
    STAMP(7);
    const int th = w & 1, kh = w >> 1;
    const int gtok = ti * 128 + tj * 8 + w * 2;
    const u32x4 g4 = *(const u32x4*)((const char*)gw + lane * 16);
    const u32x4 gx0 = *(const u32x4*)((const char*)x + (size_t)gtok * 1024 + lane * 16);
    const u32x4 gx1 = *(const u32x4*)((const char*)x + (size_t)(gtok + 1) * 1024 + lane * 16);
    SB();
    WAITL_BAR();
    STAMP(8);
    GAS char* f2 = ws + WS_F2 + (t64 * 8 + n64) * 128;
    poll16(f2, tag, ws, lane);
    STAMP(9);
    if (b == 0 && w == 0 && lane == 0) __hip_atomic_store((gu64*)(ws + WS_E), tag, RLX_AGENT);
    constexpr int PF = 11;
    u32x4 O[32];
    const unsigned obase = WS_O + ((t64 * 2 + th) * 64 + kh * 32) * 1024;
    const int orot = OROT ? (((n64 * OROT) >> 3) * 32 / OROT) : 0;
#define LD_O(n) O[n] = __builtin_amdgcn_raw_buffer_load_b128(rws, lane * 16, obase + (((n) + orot) & 31) * 1024, 16)
#pragma unroll
    for (int n = 0; n < PF; ++n) LD_O(n);
    SB();
    {
        float s0 = 0.f, s1 = 0.f;
#pragma unroll
        for (int e = 0; e < 4; ++e) {
            const float gl = __uint_as_float(g4[e] << 16), gh = __uint_as_float(g4[e] & 0xffff0000u);
            s0 = __builtin_fmaf(__uint_as_float(gx0[e] << 16), gl, s0); s0 = __builtin_fmaf(__uint_as_float(gx0[e] & 0xffff0000u), gh, s0);
            s1 = __builtin_fmaf(__uint_as_float(gx1[e] << 16), gl, s1); s1 = __builtin_fmaf(__uint_as_float(gx1[e] & 0xffff0000u), gh, s1);
        }
        s0 = wave_sum(s0); s1 = wave_sum(s1);
        const float gv = __builtin_amdgcn_rcpf(1.f + __expf(-(lane ? s1 : s0)));
        if (lane < 2) __hip_atomic_store((gu64*)(ws + WS_G + (gtok + lane) * 8), (u64)__float_as_uint(gv) | (tag << 32), RLX_AGENT);
    }
    SB();
    STAMP(12);
    f32x16 acc[2];
#pragma unroll
    for (int i = 0; i < 16; ++i) { acc[0][i] = 0.f; acc[1][i] = 0.f; }
    u64 gg;
    gu64* gptr = (gu64*)(ws + WS_G + (t64 * 64 + th * 32 + r) * 8);
    {
        const unsigned ob0 = lds0 + (kh ? L_WOB : L_WOA) + (4 * h + q4) * 128 + ((0 ^ sw) * 64) + ch * 32 + p4 * 8;
        const unsigned ob1 = ob0 ^ 64;
        bf16x8 WO[3][2];
#define RD_O1(buf, nt, n) do { ldsp p_ = (ldsp)(uintptr_t)(((nt) ? ob1 : ob0) + (unsigned)((((n) + orot) & 31) * 2048)); WO[buf][nt] = tr_frag(p_, 0, 1024); } while (0)
        RD_O1(0, 0, 0); RD_O1(0, 1, 0); RD_O1(1, 0, 1); RD_O1(1, 1, 1); SB();
#pragma unroll
        for (int n = 0; n < 32; ++n) {
            const bf16x8 of = __builtin_bit_cast(bf16x8, O[n]);
            acc[0] = MFMA(WO[n % 3][0], of, acc[0]); if (n < 30) RD_O1((n + 2) % 3, 0, n + 2);
            if (n + PF < 32) LD_O(n + PF);
            if (n + PF == 32) gg = __hip_atomic_load(gptr, RLX_AGENT);
            SB();
            acc[1] = MFMA(WO[n % 3][1], of, acc[1]); if (n < 30) RD_O1((n + 2) % 3, 1, n + 2); SB();
        }
    }
    STAMP(10);
    {
        for (unsigned spins = 0; !__all((unsigned)(gg >> 32) == (unsigned)tag); ++spins) {
            if (spins > kSpinCap) { if (lane == 0) __hip_atomic_store((gu32*)(ws + WS_TMO), 1u, RLX_AGENT); break; }
            __builtin_amdgcn_s_sleep(1);
            gg = __hip_atomic_load(gptr, RLX_AGENT);
        }
        const float g = __uint_as_float((unsigned)gg);
        const float f = kh ? 1.f - g : g;
        const int tk = 32 * th + r;
#pragma unroll
        for (int nt = 0; nt < 2; ++nt)
#pragma unroll
            for (int q = 0; q < 4; ++q) {
                const int pc = 8 * nt + 2 * q + h;
                *(LAS f32x4*)(lds + L_F + kh * 16384 + tk * 256 + ((pc ^ (tk & 15)) << 4)) =
                    (f32x4){acc[nt][4 * q] * f, acc[nt][4 * q + 1] * f, acc[nt][4 * q + 2] * f, acc[nt][4 * q + 3] * f};
            }
        WAITL_BAR();
#pragma unroll
        for (int u = 0; u < 2; ++u) {
            const int tk2 = 16 * w + 8 * u + (lane >> 3), p8 = lane & 7;
            ldsp rowp = lds + L_F + tk2 * 256;
            const int c0 = ((2 * p8) ^ (tk2 & 15)) << 4, c1 = ((2 * p8 + 1) ^ (tk2 & 15)) << 4;
            const f32x4 a0 = *(const LAS f32x4*)(rowp + c0), a1 = *(const LAS f32x4*)(rowp + c1);
            const f32x4 b0 = *(const LAS f32x4*)(rowp + 16384 + c0), b1 = *(const LAS f32x4*)(rowp + 16384 + c1);
            const f32x4 s0 = a0 + b0, s1 = a1 + b1;
            const u32x4 o = {pk2(s0[0], s0[1]), pk2(s0[2], s0[3]), pk2(s1[0], s1[1]), pk2(s1[2], s1[3])};
            *(u32x4*)((char*)out + (size_t)(t64 * 64 + tk2) * 1024 + n64 * 128 + p8 * 16) = o;
        }
    }
    STAMP(11);
    STAMP_OUT(18);
}

extern "C" void kernel_launch(void* const* d_in, const int* in_sizes, int n_in, void* d_out, int out_size, void* d_ws, size_t ws_size,
                              hipStream_t stream) {
    (void)in_sizes; (void)n_in; (void)out_size; (void)ws_size;
    (void)hipFuncSetAttribute((const void*)fused_kernel, hipFuncAttributeMaxDynamicSharedMemorySize, LDS_BYTES);
    fused_kernel<<<256, 512, LDS_BYTES, stream>>>((const u16*)d_in[0], (const u16*)d_in[1], (const u16*)d_in[2], (const u16*)d_in[4],
                                                  (const u16*)d_in[5], (const u16*)d_in[6], (u16*)d_out, (char*)d_ws);
}
```

```cpp
#include <hip/hip_runtime.h>
#include <hip/hip_bf16.h>
#include <stdint.h>

typedef unsigned short u16;
typedef unsigned long long u64;
typedef short bf16x8 __attribute__((ext_vector_type(8)));
typedef short s16x4 __attribute__((ext_vector_type(4)));
typedef float f32x16 __attribute__((ext_vector_type(16)));
typedef float f32x4 __attribute__((ext_vector_type(4)));
typedef float f32x2 __attribute__((ext_vector_type(2)));
typedef unsigned u32x4 __attribute__((ext_vector_type(4)));
typedef unsigned u32x2 __attribute__((ext_vector_type(2)));
typedef __bf16 bf16x2_t __attribute__((ext_vector_type(2)));
#define GAS __attribute__((address_space(1)))
#define LAS __attribute__((address_space(3)))
typedef GAS u64 gu64;
typedef GAS unsigned gu32;
typedef LAS char* ldsp;
#define DI __device__ __forceinline__
#define RLX_AGENT __ATOMIC_RELAXED, __HIP_MEMORY_SCOPE_AGENT
#define MFMA(a, b, c) __builtin_amdgcn_mfma_f32_32x32x16_bf16((a), (b), (c), 0, 0, 0)
#define SB() __builtin_amdgcn_sched_barrier(0)

constexpr int N_DRAIN = 8;

constexpr unsigned WS_E = 0;
constexpr unsigned WS_TMO = 256;
constexpr unsigned WS_F1 = 4096;
constexpr unsigned WS_F2 = WS_F1 + 32768;
constexpr unsigned WS_G = WS_F2 + 32768;
constexpr unsigned WS_P = WS_G + 16384;
constexpr unsigned PSZ = 2304;
constexpr unsigned WS_O = WS_P + 256 * PSZ;
constexpr unsigned WS_END = WS_O + 64 * 64 * 1024;

constexpr unsigned L_X = 0;
constexpr unsigned L_W = 49152;
constexpr unsigned L_WQ = 98304;
constexpr unsigned L_EX = 65536;
constexpr unsigned L_EXZ = 81920;
constexpr unsigned L_CTL = 86016;
constexpr unsigned L_KVZ = 90112;
constexpr unsigned L_F = 65536;
constexpr unsigned L_WOA = 0, L_WOB = 98304;
constexpr unsigned LDS_BYTES = 163840;
constexpr unsigned kSpinCap = 1u << 18;

DI unsigned pk2(float a, float b) { f32x2 v = {a, b}; return __builtin_bit_cast(unsigned, __builtin_convertvector(v, bf16x2_t)); }
constexpr float ONEP = 1.000001f, LOG2E = 1.4426950408889634f;
typedef short s16x2 __attribute__((ext_vector_type(2)));
DI unsigned pkphi(float a0, float a1) {
    const f32x2 a = {a0, a1};
    const f32x2 u = __builtin_elementwise_fma(a, (f32x2){LOG2E, LOG2E}, (f32x2){-ONEP * LOG2E, -ONEP * LOG2E});
    const f32x2 e = {__builtin_amdgcn_fmed3f(__builtin_amdgcn_exp2f(u[0]), 0.f, 1.f), __builtin_amdgcn_fmed3f(__builtin_amdgcn_exp2f(u[1]), 0.f, 1.f)};
    const f32x2 c = e + 1e-6f;
    const unsigned pa = pk2(a0, a1), pc = pk2(c[0], c[1]);
    return __builtin_bit_cast(unsigned, __builtin_elementwise_max(__builtin_bit_cast(s16x2, pa), __builtin_bit_cast(s16x2, pc)));
}
DI bf16x8 pack8(const f32x16& x, int s) {
    u32x4 p = {pk2(x[8 * s], x[8 * s + 1]), pk2(x[8 * s + 2], x[8 * s + 3]), pk2(x[8 * s + 4], x[8 * s + 5]), pk2(x[8 * s + 6], x[8 * s + 7])};
    return __builtin_bit_cast(bf16x8, p);
}
DI bf16x8 pack8phi(const f32x16& x, int s) {
    u32x4 p = {pkphi(x[8 * s], x[8 * s + 1]), pkphi(x[8 * s + 2], x[8 * s + 3]), pkphi(x[8 * s + 4], x[8 * s + 5]), pkphi(x[8 * s + 6], x[8 * s + 7])};
    return __builtin_bit_cast(bf16x8, p);
}
DI float dpp_add(float t, int ctrl_is_ror8) {
    const int i = __builtin_bit_cast(int, t);
    return t + __builtin_bit_cast(float, ctrl_is_ror8 ? __builtin_amdgcn_update_dpp(0, i, 0x128, 0xf, 0xf, true) : __builtin_amdgcn_update_dpp(0, i, 0x124, 0xf, 0xf, true));
}
DI float rows_sum(float t) {
    auto r16 = __builtin_amdgcn_permlane16_swap(__float_as_uint(t), __float_as_uint(t), false, false);
    t = __uint_as_float(r16[0]) + __uint_as_float(r16[1]);
    auto r32 = __builtin_amdgcn_permlane32_swap(__float_as_uint(t), __float_as_uint(t), false, false);
    return __uint_as_float(r32[0]) + __uint_as_float(r32[1]);
}
DI float wave_sum(float t) {
    int i = __builtin_bit_cast(int, t);
    t += __builtin_bit_cast(float, __builtin_amdgcn_update_dpp(0, i, 0x121, 0xf, 0xf, true)); i = __builtin_bit_cast(int, t);
    t += __builtin_bit_cast(float, __builtin_amdgcn_update_dpp(0, i, 0x122, 0xf, 0xf, true));
    t = dpp_add(t, 0); t = dpp_add(t, 1);
    return rows_sum(t);
}
DI bf16x8 lds_frag(ldsp p) { return *(const LAS bf16x8*)p; }
typedef short v4i16_t __attribute__((ext_vector_type(4)));
DI s16x4 vtr(ldsp p) { return __builtin_bit_cast(s16x4, __builtin_amdgcn_ds_read_tr16_b64_v4i16((LAS v4i16_t*)p)); }
DI bf16x8 tr_frag(ldsp p, int off0, int off1) {
    s16x4 lo = vtr(p + off0), hi = vtr(p + off1);
    return __builtin_shufflevector(lo, hi, 0, 1, 2, 3, 4, 5, 6, 7);
}
template <int IMM> DI void blds(__amdgpu_buffer_rsrc_t r, unsigned voff, unsigned soff, unsigned ldsb) {
    asm volatile("s_add_i32 m0, %3, %4\n\ts_nop 0\n\tbuffer_load_dwordx4 %0, %1, %2 offen lds" :: "v"(voff), "s"(r), "s"(soff), "s"(ldsb), "i"(IMM) : "memory", "scc", "m0");
}
DI void gld_sc1(u32x4& d, __amdgpu_buffer_rsrc_t r, unsigned voff, unsigned soff) {
    asm volatile("buffer_load_dwordx4 %0, %1, %2, %3 offen sc1" : "=v"(d) : "v"(voff), "s"(r), "s"(soff) : "memory");
}
DI void gld_plain(u32x4& d, const void* sbase, unsigned voff) {
    asm volatile("global_load_dwordx4 %0, %1, %2" : "=v"(d) : "v"(voff), "s"(sbase) : "memory");
}
#define WAITV_BAR(N) asm volatile("s_waitcnt vmcnt(" #N ")\n\ts_barrier" ::: "memory")
#define WAITL_BAR() asm volatile("s_waitcnt lgkmcnt(0)\n\ts_barrier" ::: "memory")
#define DRAIN() asm volatile("s_waitcnt vmcnt(0)" ::: "memory")

DI u64 flag_ld(GAS char* line, int lane) { return __hip_atomic_load((gu64*)(line + (lane & 15) * 8), RLX_AGENT); }
DI void poll16(GAS char* line, u64 tag, GAS char* ws, int lane) {
    for (unsigned spins = 0;; ++spins) {
        const u64 a = flag_ld(line, lane);
        if (__all(a == tag)) break;
        if (spins > kSpinCap) { if (lane == 0) __hip_atomic_store((gu32*)(ws + WS_TMO), 1u, RLX_AGENT); break; }
        __builtin_amdgcn_s_sleep(1);
    }
    asm volatile("" ::: "memory");
}

DI void final_rows(ldsp lds, __amdgpu_buffer_rsrc_t rout, unsigned obase_out, int w8, int lane) {
    const int tk2 = 8 * w8 + (lane >> 3), p8 = lane & 7;
    ldsp p0 = lds + L_F + tk2 * 256 + (((2 * p8) ^ (tk2 & 15)) << 4), p1 = lds + L_F + tk2 * 256 + (((2 * p8 + 1) ^ (tk2 & 15)) << 4);
    asm volatile("" : "+v"(p0), "+v"(p1));
    WAITL_BAR();
    const f32x4 a0 = *(const LAS f32x4*)p0, a1 = *(const LAS f32x4*)p1, b0 = *(const LAS f32x4*)(p0 + 16384), b1 = *(const LAS f32x4*)(p1 + 16384);
    const f32x4 s0 = a0 + b0, s1 = a1 + b1;
    const u32x4 o = {pk2(s0[0], s0[1]), pk2(s0[2], s0[3]), pk2(s1[0], s1[1]), pk2(s1[2], s1[3])};
    __builtin_amdgcn_raw_buffer_store_b128(o, rout, (unsigned)(tk2 * 1024 + p8 * 16), obase_out, 16);
}

__global__ __launch_bounds__(512, 2) void fused_kernel(const u16* __restrict__ x, const u16* __restrict__ wA, const u16* __restrict__ woA,
                                                       const u16* __restrict__ wB, const u16* __restrict__ woB, const u16* __restrict__ gw,
                                                       u16* __restrict__ out, char* wsp) {
    extern __shared__ __attribute__((aligned(16))) char smem[];
    GAS char* ws = (GAS char*)wsp;
    const ldsp lds = (ldsp)smem;
    const unsigned lds0 = (unsigned)(uintptr_t)lds;
    const int wave = __builtin_amdgcn_readfirstlane(threadIdx.x >> 6), lane = threadIdx.x & 63;
    const int b = blockIdx.x, xcd = b & 7, idx = b >> 3, il = idx & 7, jl = idx >> 3;
    const int ti = (xcd & 1) * 8 + il, tj = (xcd >> 1) * 4 + jl;
    const int rot = 4 * ((il + jl) & 1);
    const int t64 = xcd * 4 + (idx & 3), n64 = idx >> 2;
    const __amdgpu_buffer_rsrc_t rws = __builtin_amdgcn_make_buffer_rsrc(wsp, 0, WS_END, 0x00020000);
    const __amdgpu_buffer_rsrc_t rout = __builtin_amdgcn_make_buffer_rsrc(out, 0, 2048 * 1024, 0x00020000);
    const unsigned obase_out = t64 * 65536 + n64 * 128;
    asm volatile("" :: "s"(x), "s"(wA), "s"(wB), "s"(woA), "s"(woB), "s"(wsp), "s"(out), "s"(gw));

    if (__builtin_expect(wave >= 4, 1)) {
        const int lw = wave - 4;
        __builtin_amdgcn_s_setprio(0);
        const unsigned woff = (tj & 7) * 128 + lw * 16 * 3072, xoff = (ti * 128 + lw * 32) * 1024;
        const __amdgpu_buffer_rsrc_t rw = __builtin_amdgcn_make_buffer_rsrc((char*)((tj >> 3) ? wB : wA) + woff, 0, 512 * 3072 - woff, 0x00020000);
        const __amdgpu_buffer_rsrc_t rx = __builtin_amdgcn_make_buffer_rsrc((char*)x + xoff, 0, 2048 * 1024 - xoff, 0x00020000);
        const unsigned vx0 = (lane >> 3) * 1024 + (((lane & 7) ^ (lane >> 4)) << 4), vx1 = vx0 ^ 64;
        const unsigned vw = (lane >> 3) * 3072 + (((lane & 7) ^ (((lane >> 4) & 1) << 2)) << 4);
        const unsigned lx = lds0 + lw * 4096, lw2 = lds0 + lw * 2048;
        unsigned vo = (lane >> 3) * 1024 + (((lane & 7) ^ (((lane >> 4) & 1) << 2)) << 4);
        asm volatile("" : "+v"(vo));
#define ISSUE_KV(S) do { const unsigned cc_ = ((S) + rot) & 7; \
            blds<L_X + (((S) + 2) % 3) * 16384>(rx, vx0, cc_ * 128, lx); blds<L_X + (((S) + 2) % 3) * 16384 + 1024>(rx, vx1, cc_ * 128 + 8192, lx); \
            blds<L_X + (((S) + 2) % 3) * 16384 + 2048>(rx, vx0, cc_ * 128 + 16384, lx); blds<L_X + (((S) + 2) % 3) * 16384 + 3072>(rx, vx1, cc_ * 128 + 24576, lx); \
            blds<L_W + (((S) + 2) % 3) * 16384>(rw, vw, cc_ * (64 * 3072) + 1024, lw2); blds<L_W + (((S) + 2) % 3) * 16384 + 1024>(rw, vw, cc_ * (64 * 3072) + 8 * 3072 + 1024, lw2); \
            blds<L_W + (((S) + 2) % 3) * 16384 + 8192>(rw, vw, cc_ * (64 * 3072) + 2048, lw2); blds<L_W + (((S) + 2) % 3) * 16384 + 8192 + 1024>(rw, vw, cc_ * (64 * 3072) + 8 * 3072 + 2048, lw2); } while (0)
#define ISSUE_Q(S) do { const unsigned cc_ = ((S) + rot) & 7; \
            blds<L_WQ + (S) * 8192>(rw, vw, cc_ * (64 * 3072), lw2); blds<L_WQ + (S) * 8192 + 1024>(rw, vw, cc_ * (64 * 3072) + 8 * 3072, lw2); } while (0)
        SB();
        ISSUE_KV(0); ISSUE_KV(1);
        const int gtok = ti * 128 + tj * 8 + lw * 2;
        u32x4 g4, gx0, gx1;
        gld_plain(g4, gw, lane * 16);
        gld_plain(gx0, (const char*)x + (size_t)gtok * 1024, lane * 16);
        gld_plain(gx1, (const char*)x + (size_t)(gtok + 1) * 1024, lane * 16);
        SB();
        unsigned vg = (lane >> 5) * PSZ + (lane & 31) * 16, vz = (lane >> 2) * PSZ + (lane & 3) * 16, vf = (lane & 15) * 8;
        bf16x8 sel;
        {
            const unsigned m = lane & 31, one = (m < 8) ? (0x3F80u << (16 * (m & 1))) : 0u;
            sel = __builtin_bit_cast(bf16x8, (u32x4){(m >> 1) == 0 ? one : 0u, (m >> 1) == 1 ? one : 0u, (m >> 1) == 2 ? one : 0u, (m >> 1) == 3 ? one : 0u});
        }
        ldsp kvd = lds + L_KVZ + lw * 1024 + (lane & 31) * 16 + (lane >> 5) * 8, kzd = lds + L_KVZ + lw * 1024 + 512 + (lane & 1) * 16 + ((lane >> 1) & 1) * 8;
        asm volatile("" : "+v"(vg), "+v"(vz), "+v"(vf), "+v"(sel), "+v"(kvd), "+v"(kzd));
        u64 epoch;
        asm volatile("s_load_dwordx2 %0, %1, 0x0" : "=s"(epoch) : "s"(wsp) : "memory");
        WAITV_BAR(11);
        ISSUE_KV(2);
        WAITV_BAR(11);  ISSUE_KV(3);
        WAITV_BAR(8);
        asm volatile("" : "+v"(g4), "+v"(gx0), "+v"(gx1));
        ISSUE_KV(4);
        WAITV_BAR(8);  ISSUE_KV(5);
        WAITV_BAR(8);  ISSUE_KV(6);
        WAITV_BAR(8);  ISSUE_KV(7);
        WAITV_BAR(8);
        ISSUE_Q(0); ISSUE_Q(1); ISSUE_Q(2); ISSUE_Q(3);
        WAITV_BAR(8);
        ISSUE_Q(4); ISSUE_Q(5); ISSUE_Q(6); ISSUE_Q(7);
        const __amdgpu_buffer_rsrc_t roa = __builtin_amdgcn_make_buffer_rsrc((char*)woA + n64 * 128, 0, 512 * 1024 - n64 * 128, 0x00020000);
        const __amdgpu_buffer_rsrc_t rob = __builtin_amdgcn_make_buffer_rsrc((char*)woB + n64 * 128, 0, 512 * 1024 - n64 * 128, 0x00020000);
#define WO1(R, LDSB, n0, u) do { const unsigned n_ = (n0) + (((u) + 2 * (idx & 3)) & 7); blds<LDSB>(R, vo, n_ * 8192, lds0 + n_ * 1024); } while (0)
#define ISSUE_WO8(R, LDSB, n0) do { WO1(R, LDSB, n0, 0); WO1(R, LDSB, n0, 1); WO1(R, LDSB, n0, 2); WO1(R, LDSB, n0, 3); WO1(R, LDSB, n0, 4); WO1(R, LDSB, n0, 5); WO1(R, LDSB, n0, 6); WO1(R, LDSB, n0, 7); } while (0)
        ISSUE_WO8(roa, L_WOA, 16 + lw * 8);
        asm volatile("s_waitcnt lgkmcnt(0)" : "+s"(epoch) :: "memory");
        const u64 tag = epoch + 1;
        if (lw == 0 && lane == 0) *(LAS u64*)(lds + L_CTL + 16) = tag;
        asm volatile("s_waitcnt lgkmcnt(0)" ::: "memory");
        WAITV_BAR(8);
        WAITV_BAR(63);
        ISSUE_WO8(roa, L_WOA, (lw < 2 ? 0 : 32) + lw * 8);
        WAITV_BAR(63);
        ISSUE_WO8(rob, L_WOB, lw * 8);
        {
            GAS char* f1 = ws + WS_F1 + (tj * 16 + ti) * 128;
            for (unsigned spins = 0;; ++spins) {
                const u64 v = __hip_atomic_load((gu64*)(f1 + vf), RLX_AGENT);
                if (__all(v == tag)) break;
                if (spins > kSpinCap) { if (lane == 0) __hip_atomic_store((gu32*)(ws + WS_TMO), 1u, RLX_AGENT); break; }
                __builtin_amdgcn_s_sleep(1);
            }
            asm volatile("" ::: "memory");
            const unsigned pb = WS_P + tj * 16 * PSZ;
            u32x4 gz, gv[8];
            gld_sc1(gz, rws, vz, pb + 2048 + lw * 64);
#pragma unroll
            for (int q = 0; q < 8; ++q) gld_sc1(gv[q], rws, vg, pb + 2 * q * PSZ + lw * 512);
            asm volatile("s_barrier" ::: "memory");
            ISSUE_WO8(rob, L_WOB, 32 + lw * 8);
            asm volatile("s_waitcnt vmcnt(8)" : "+v"(gz), "+v"(gv[0]), "+v"(gv[1]), "+v"(gv[2]), "+v"(gv[3]), "+v"(gv[4]), "+v"(gv[5]), "+v"(gv[6]), "+v"(gv[7]) :: "memory");
            SB();
            f32x4 z = __builtin_bit_cast(f32x4, gz);
#pragma unroll
            for (int e = 0; e < 4; ++e) z[e] = rows_sum(dpp_add(dpp_add(z[e], 0), 1));
            if (lane < 4) *(LAS u32x2*)kzd = (u32x2){pk2(z[0], z[1]), pk2(z[2], z[3])};
            SB();
            f32x16 c;
#pragma unroll
            for (int i = 0; i < 16; ++i) c[i] = 0.f;
#pragma unroll
            for (int q = 0; q < 8; ++q) c = MFMA(sel, __builtin_bit_cast(bf16x8, gv[q]), c);
            *(LAS u32x2*)kvd = (u32x2){pk2(c[0], c[1]), pk2(c[2], c[3])};
        }
        WAITL_BAR();
        asm volatile("s_barrier" ::: "memory");
        {
            float s0 = 0.f, s1 = 0.f;
#pragma unroll
            for (int e = 0; e < 4; ++e) {
                const float gl = __uint_as_float(g4[e] << 16), gh = __uint_as_float(g4[e] & 0xffff0000u);
                s0 = __builtin_fmaf(__uint_as_float(gx0[e] << 16), gl, s0); s0 = __builtin_fmaf(__uint_as_float(gx0[e] & 0xffff0000u), gh, s0);
                s1 = __builtin_fmaf(__uint_as_float(gx1[e] << 16), gl, s1); s1 = __builtin_fmaf(__uint_as_float(gx1[e] & 0xffff0000u), gh, s1);
            }
            s0 = wave_sum(s0); s1 = wave_sum(s1);
            const float gv = __builtin_amdgcn_rcpf(1.f + __expf(-(lane ? s1 : s0)));
            if (lane < 2) __hip_atomic_store((gu64*)(ws + WS_G + (gtok + lane) * 8), (u64)__float_as_uint(gv) | (tag << 32), RLX_AGENT);
        }
        WAITV_BAR(0);
        final_rows(lds, rout, obase_out, wave, lane);
        return;
    }

    const int w = wave, r = lane & 31, h = lane >> 5;
    const int q4 = (lane & 15) >> 2, p4 = lane & 3, ch = (lane >> 4) & 1, sw = (q4 >> 1) & 1;
    ldsp xa[4];
    {
        const int row = 32 * w + r;
        const unsigned a0 = row * 128 + ((h ^ ((row >> 1) & 7)) << 4);
#pragma unroll
        for (int ks = 0; ks < 4; ++ks) xa[ks] = lds + L_X + (a0 ^ (ks << 5));
    }
    ldsp wb[2];
#pragma unroll
    for (int ct = 0; ct < 2; ++ct) wb[ct] = lds + L_W + (8 * h + q4) * 128 + ((ct ^ sw) * 64) + ch * 32 + p4 * 8;

    bf16x8 X[32];
    f32x16 aK[2], aV[2];
#pragma unroll
    for (int i = 0; i < 16; ++i) { aK[0][i] = ONEP; aK[1][i] = ONEP; aV[0][i] = 0.f; aV[1][i] = 0.f; }
    bf16x8 WK[2][2], WV[2][2];
#define OFFW(s, ks) ((((s) + 2) % 3) * 16384 + (ks) * 2048)
#define RD_K(buf, ct, s, ks) WK[buf][ct] = tr_frag(wb[ct], OFFW(s, ks), OFFW(s, ks) + 512)
#define RD_V(buf, ct, s, ks) WV[buf][ct] = tr_frag(wb[ct], OFFW(s, ks) + 8192, OFFW(s, ks) + 8192 + 512)
#define RD_X1(s, ks) X[4 * (s) + (ks)] = lds_frag(xa[ks] + (((s) + 2) % 3) * 16384)
    WAITL_BAR();
    RD_X1(0, 0); RD_K(0, 0, 0, 0); RD_V(0, 0, 0, 0); RD_K(0, 1, 0, 0); RD_V(0, 1, 0, 0); RD_X1(0, 1); RD_X1(0, 2); RD_X1(0, 3);
    SB();
#pragma unroll
    for (int n = 0; n < 32; ++n) {
        const int ks = n & 3, cur = n & 1, nxt = cur ^ 1;
        const int s2 = (n + 1) >> 2, ks2 = (n + 1) & 3;
        if (ks == 3 && n < 31) { WAITL_BAR(); }
        aK[0] = MFMA(X[n], WK[cur][0], aK[0]); if (n < 31) { RD_K(nxt, 0, s2, ks2); RD_V(nxt, 0, s2, ks2); } SB();
        aV[0] = MFMA(X[n], WV[cur][0], aV[0]); if (n < 31) { RD_K(nxt, 1, s2, ks2); RD_V(nxt, 1, s2, ks2); } SB();
        aK[1] = MFMA(X[n], WK[cur][1], aK[1]); if (n < 31 && ks == 3) { RD_X1(s2, 0); RD_X1(s2, 1); } SB();
        aV[1] = MFMA(X[n], WV[cur][1], aV[1]); if (n < 31 && ks == 3) { RD_X1(s2, 2); RD_X1(s2, 3); } SB();
    }
    {
        const bf16x8 ones = {0x3F80, 0x3F80, 0x3F80, 0x3F80, 0x3F80, 0x3F80, 0x3F80, 0x3F80};
        f32x16 zero;
#pragma unroll
        for (int i = 0; i < 16; ++i) zero[i] = 0.f;
        f32x16 ckv[2], cz[2];
        bf16x8 pk = pack8phi(aK[0], 0), vv = pack8(aV[0], 0);
        ckv[0] = MFMA(pk, vv, zero); cz[0] = MFMA(ones, pk, zero); SB();
        pk = pack8phi(aK[0], 1); vv = pack8(aV[0], 1);
        ckv[0] = MFMA(pk, vv, ckv[0]); cz[0] = MFMA(ones, pk, cz[0]); SB();
        pk = pack8phi(aK[1], 0); vv = pack8(aV[1], 0);
        ckv[1] = MFMA(pk, vv, zero); cz[1] = MFMA(ones, pk, zero); SB();
        pk = pack8phi(aK[1], 1); vv = pack8(aV[1], 1);
        const bool hi = (lane >> 4) & 1;
        ldsp dst = lds + L_EX + w * 4096 + (hi ? 1024 : 0) + (h * 16 + (lane & 15)) * 16;
        {
            const f32x16& c = ckv[0];
            *(LAS f32x4*)(dst) = hi ? (f32x4){c[8], c[9], c[10], c[11]} : (f32x4){c[0], c[1], c[2], c[3]};
            *(LAS f32x4*)(dst + 512) = hi ? (f32x4){c[12], c[13], c[14], c[15]} : (f32x4){c[4], c[5], c[6], c[7]};
            if (lane < 32) *(LAS float*)(lds + L_EXZ + w * 256 + lane * 4) = cz[0][0];
        }
        SB();
        ckv[1] = MFMA(pk, vv, ckv[1]); cz[1] = MFMA(ones, pk, cz[1]);
        if (w == 0 && lane < 3) *(LAS unsigned*)(lds + L_CTL + lane * 4) = 0u;
        {
            const f32x16& c = ckv[1];
            *(LAS f32x4*)(dst + 2048) = hi ? (f32x4){c[8], c[9], c[10], c[11]} : (f32x4){c[0], c[1], c[2], c[3]};
            *(LAS f32x4*)(dst + 2048 + 512) = hi ? (f32x4){c[12], c[13], c[14], c[15]} : (f32x4){c[4], c[5], c[6], c[7]};
            if (lane < 32) *(LAS float*)(lds + L_EXZ + w * 256 + 128 + lane * 4) = cz[1][0];
        }
    }
    WAITL_BAR();
    u64 tag;
    {
        const u32x2 tv = *(const LAS u32x2*)(lds + L_CTL + 16);
        tag = (u64)(unsigned)__builtin_amdgcn_readfirstlane(tv[0]) | ((u64)(unsigned)__builtin_amdgcn_readfirstlane(tv[1]) << 32);
    }
    {
        f32x4 a = *(const LAS f32x4*)(lds + L_EX + w * 1024 + lane * 16);
        float z = *(const LAS float*)(lds + L_EXZ + w * 64 + (lane & 15) * 4);
#pragma unroll
        for (int v = 1; v < 4; ++v) {
            a += *(const LAS f32x4*)(lds + L_EX + v * 4096 + w * 1024 + lane * 16);
            z += *(const LAS float*)(lds + L_EXZ + v * 256 + w * 64 + (lane & 15) * 4);
        }
        const unsigned pb = WS_P + (tj * 16 + ti) * PSZ;
        __builtin_amdgcn_raw_buffer_store_b64((u32x2){pk2(a[0], a[1]), pk2(a[2], a[3])}, rws, (lane & 31) * 16 + (lane >> 5) * 8, pb + w * 512, 16);
        if (lane < 16) __builtin_amdgcn_raw_buffer_store_b32(__builtin_bit_cast(unsigned, z), rws, lane * 4, pb + 2048 + w * 64, 16);
    }
    asm volatile("s_barrier" ::: "memory");
    SB();
    f32x16 aQ[2];
#pragma unroll
    for (int i = 0; i < 16; ++i) { aQ[0][i] = ONEP; aQ[1][i] = ONEP; }
    {
        ldsp wq0 = wb[0] + (L_WQ - L_W), wq1 = wb[1] + (L_WQ - L_W);
        asm volatile("" : "+v"(wq0), "+v"(wq1));
        bf16x8 WQ[3][2];
#define RD_Q1(buf, ct, n) WQ[buf][ct] = tr_frag((ct) ? wq1 : wq0, ((n) >> 2) * 8192 + ((n) & 3) * 2048, ((n) >> 2) * 8192 + ((n) & 3) * 2048 + 512)
        RD_Q1(0, 0, 0); RD_Q1(0, 1, 0); RD_Q1(1, 0, 1); RD_Q1(1, 1, 1); SB();
        unsigned old = 0;
#pragma unroll
        for (int n = 0; n < 32; ++n) {
            if (n == 16) { asm volatile("s_barrier" ::: "memory"); SB(); }
            if (n == N_DRAIN) {
                DRAIN();
                if (lane == 0) old = __hip_atomic_fetch_add((LAS unsigned*)(lds + L_CTL), 1u, __ATOMIC_RELAXED, __HIP_MEMORY_SCOPE_WORKGROUP);
                SB();
            }
            if (n == N_DRAIN + 2) {
                if (__builtin_amdgcn_readfirstlane(old) == 3 && lane < 16) __hip_atomic_store((gu64*)(ws + WS_F1 + (tj * 16 + lane) * 128 + ti * 8), tag, RLX_AGENT);
                SB();
            }
            aQ[0] = MFMA(WQ[n % 3][0], X[n], aQ[0]); if (n < 30) RD_Q1((n + 2) % 3, 0, n + 2); SB();
            aQ[1] = MFMA(WQ[n % 3][1], X[n], aQ[1]); if (n < 30) RD_Q1((n + 2) % 3, 1, n + 2); SB();
        }
    }
    WAITL_BAR();
    bf16x8 PQ[4];
#pragma unroll
    for (int hd = 0; hd < 4; ++hd) PQ[hd] = pack8phi(aQ[hd >> 1], hd & 1);
    asm volatile("" : "+v"(PQ[0]), "+v"(PQ[1]), "+v"(PQ[2]), "+v"(PQ[3]));
    SB();
    WAITL_BAR();
    {
        ldsp ka = lds + L_KVZ + (r < 16 ? (h * 16 + r) * 16 : 512 + h * 16);
        f32x16 zero;
#pragma unroll
        for (int i = 0; i < 16; ++i) zero[i] = 0.f;
        f32x16 c[4];
#pragma unroll
        for (int hd = 0; hd < 4; ++hd) c[hd] = MFMA(lds_frag(ka + hd * 1024), PQ[hd], zero);
        SB();
#pragma unroll
        for (int hd = 0; hd < 4; ++hd) {
            const float inv = __builtin_amdgcn_rcpf(c[hd][8] + 1e-6f);
            const u32x4 o = {pk2(c[hd][0] * inv, c[hd][1] * inv), pk2(c[hd][2] * inv, c[hd][3] * inv), pk2(c[hd][4] * inv, c[hd][5] * inv), pk2(c[hd][6] * inv, c[hd][7] * inv)};
            __builtin_amdgcn_raw_buffer_store_b128(o, rws, lane * 16, WS_O + ((ti * 4 + w) * 64 + tj * 4 + hd) * 1024, 16);
            SB();
        }
        asm volatile("s_barrier" ::: "memory");
        DRAIN();
        unsigned old = 0;
        if (lane == 0) old = __hip_atomic_fetch_add((LAS unsigned*)(lds + L_CTL + 4), 1u, __ATOMIC_RELAXED, __HIP_MEMORY_SCOPE_WORKGROUP);
        if (__builtin_amdgcn_readfirstlane(old) == 3 && lane < 16) __hip_atomic_store((gu64*)(ws + WS_F2 + (ti * 16 + lane) * 128 + tj * 8), tag, RLX_AGENT);
    }
    const int th = w & 1, kh = w >> 1;
    GAS char* f2 = ws + WS_F2 + (t64 * 8 + n64) * 128;
    WAITL_BAR();
    poll16(f2, tag, ws, lane);
    if (b == 0 && w == 0 && lane == 0) __hip_atomic_store((gu64*)(ws + WS_E), tag, RLX_AGENT);
    constexpr int PF = 11;
    u32x4 O[32];
    const unsigned obase = WS_O + ((t64 * 2 + th) * 64 + kh * 32) * 1024;
#define LD_O(n) O[n] = __builtin_amdgcn_raw_buffer_load_b128(rws, lane * 16, obase + (n) * 1024, 16)
#pragma unroll
    for (int n = 0; n < PF; ++n) LD_O(n);
    SB();
    f32x16 acc[2];
#pragma unroll
    for (int i = 0; i < 16; ++i) { acc[0][i] = 0.f; acc[1][i] = 0.f; }
    u64 gg;
    float gf;
    ldsp fa[8];
    gu64* gptr = (gu64*)(ws + WS_G + (t64 * 64 + th * 32 + r) * 8);
    {
        const unsigned ob0 = lds0 + (kh ? L_WOB : L_WOA) + (4 * h + q4) * 128 + ((0 ^ sw) * 64) + ch * 32 + p4 * 8;
        const unsigned ob1 = ob0 ^ 64;
        bf16x8 WO[3][2];
#define RD_O1(buf, nt, n) do { ldsp p_ = (ldsp)(uintptr_t)(((nt) ? ob1 : ob0) + (unsigned)((n) * 2048)); WO[buf][nt] = tr_frag(p_, 0, 1024); } while (0)
        RD_O1(0, 0, 0); RD_O1(0, 1, 0); RD_O1(1, 0, 1); RD_O1(1, 1, 1); SB();
        {
            const unsigned tk = 32 * th + r, f0 = (L_F + kh * 16384 + tk * 256 + ((tk & 15) << 4)) ^ (h << 4);
#pragma unroll
            for (int i = 0; i < 8; ++i) { fa[i] = lds + (f0 ^ ((8 * (i >> 2) + 2 * (i & 3)) << 4)); asm volatile("" : "+v"(fa[i])); }
        }
        SB();
#pragma unroll
        for (int n = 0; n < 32; ++n) {
            if (n == 30) {
                for (unsigned spins = 0; !__all((unsigned)(gg >> 32) == (unsigned)tag); ++spins) {
                    if (spins > kSpinCap) { if (lane == 0) __hip_atomic_store((gu32*)(ws + WS_TMO), 1u, RLX_AGENT); break; }
                    __builtin_amdgcn_s_sleep(1);
                    gg = __hip_atomic_load(gptr, RLX_AGENT);
                }
                const float g = __uint_as_float((unsigned)gg);
                gf = kh ? 1.f - g : g;
                asm volatile("" : "+v"(gf));
                SB();
            }
            const bf16x8 of = __builtin_bit_cast(bf16x8, O[n]);
            acc[0] = MFMA(WO[n % 3][0], of, acc[0]); if (n < 30) RD_O1((n + 2) % 3, 0, n + 2);
            if (n + PF < 32) LD_O(n + PF);
            if (n + PF == 32) gg = __hip_atomic_load(gptr, RLX_AGENT);
            SB();
            acc[1] = MFMA(WO[n % 3][1], of, acc[1]); if (n < 30) RD_O1((n + 2) % 3, 1, n + 2); SB();
        }
    }
    {
        const f32x2 f2v = {gf, gf};
#pragma unroll
        for (int nt = 0; nt < 2; ++nt)
#pragma unroll
            for (int q = 0; q < 4; ++q) {
                const f32x2 lo = (f32x2){acc[nt][4 * q], acc[nt][4 * q + 1]} * f2v, hi = (f32x2){acc[nt][4 * q + 2], acc[nt][4 * q + 3]} * f2v;
                *(LAS f32x4*)fa[4 * nt + q] = (f32x4){lo[0], lo[1], hi[0], hi[1]};
            }
        final_rows(lds, rout, obase_out, w, lane);
    }
}

extern "C" void kernel_launch(void* const* d_in, const int* in_sizes, int n_in, void* d_out, int out_size, void* d_ws, size_t ws_size,
                              hipStream_t stream) {
    (void)in_sizes; (void)n_in; (void)out_size; (void)ws_size;
    (void)hipFuncSetAttribute((const void*)fused_kernel, hipFuncAttributeMaxDynamicSharedMemorySize, LDS_BYTES);
    fused_kernel<<<256, 512, LDS_BYTES, stream>>>((const u16*)d_in[0], (const u16*)d_in[1], (const u16*)d_in[2], (const u16*)d_in[4],
                                                  (const u16*)d_in[5], (const u16*)d_in[6], (u16*)d_out, (char*)d_ws);
}
```
